```python
import torch

def _softmax(x, axis=-1):
    return torch.softmax(x, dim=axis)
def _gelu(x):
    return torch.nn.functional.gelu(x, approximate="tanh")
def _silu(x):
    return torch.nn.functional.silu(x)
def _sigmoid(x):
    return torch.sigmoid(x)
def _layernorm(x, axis=-1, eps=1e-5):
    m = x.mean(dim=axis, keepdim=True)
    v = x.var(dim=axis, keepdim=True, unbiased=False)
    return (x - m) / torch.sqrt(v + eps)
def _scatter_add(tgt, idx, src, axis=0):
    return tgt.clone().index_add_(axis, idx, src)
def _segment_sum(src, seg, n):
    out = torch.zeros((n,) + src.shape[1:], dtype=src.dtype)
    return out.index_add_(0, seg, src)
def _pad(x, pw):
    flat = tuple(v for pair in reversed(pw) for v in pair)
    return torch.nn.functional.pad(x, flat)

kernel_name = 'gather_scatter__hybrid_graph_tf_nagphormer__graph'

def setup_inputs():

    _w = (lambda a: (lambda t: t.to(torch.bfloat16) if t.is_floating_point() else t)(torch.as_tensor(a)))
    N, deg, D = 10000, 32, 128
    E = N * deg
    src = (lambda lo, hi, *s: torch.randint(lo, hi, s))(0, N, E).to(torch.int64)
    dst = (lambda lo, hi, *s: torch.randint(lo, hi, s))(0, N, E).to(torch.int64)
    deg_cnt = torch.bincount(dst, minlength=N).to(float) + 1.0
    sc = 1.0 / D ** 0.5
    return {
        "x": _w((lambda *s: torch.randn(*s, dtype=torch.bfloat16) * 0.1)(N, D)),
        "src": _w(src),
        "dst": _w(dst),
        "deg_inv_sqrt": _w(1.0 / (lambda _x: torch.sqrt(torch.as_tensor(_x)))(deg_cnt)),
        "wq": _w((lambda *s: torch.randn(*s, dtype=torch.bfloat16) * 1.6)(D, D) * sc),
        "wk": _w((lambda *s: torch.randn(*s, dtype=torch.bfloat16) * 1.6)(D, D) * sc),
        "wv": _w((lambda *s: torch.randn(*s, dtype=torch.bfloat16) * 0.1)(D, D) * sc),
        "wo": _w((lambda *s: torch.randn(*s, dtype=torch.bfloat16) * 0.1)(D, D) * sc),
        "w_r": _w((lambda *s: torch.randn(*s, dtype=torch.bfloat16) * 0.1)(D)),
    }

def reference(x, src, dst, deg_inv_sqrt, wq, wk, wv, wo, w_r):
    N, D = x.shape[0], x.shape[1]
    K = 7
    h = x
    toks = [x]
    for _ in range(K):
        msg = h[src] * deg_inv_sqrt[src, None] * deg_inv_sqrt[dst, None]
        h = _segment_sum(msg, dst, N)
        toks.append(h)
    tok = torch.stack(toks, axis=1)
    t = _layernorm(tok, axis=-1)
    q   = torch.einsum('nkd,de->nke', t, wq)
    key = torch.einsum('nkd,de->nke', t, wk)
    v   = torch.einsum('nkd,de->nke', t, wv)
    scale = 1.0 / (D ** 0.5)
    attn = _softmax(torch.einsum('nkd,nld->nkl', q, key) * scale, axis=-1)
    o = torch.einsum('nkl,nld->nkd', attn, v)
    o = tok + torch.einsum('nkd,de->nke', o, wo)
    r = _layernorm(o, axis=-1)
    alpha = _softmax(torch.einsum('nkd,d->nk', r, w_r), axis=-1)
    return torch.einsum('nk,nkd->nd', alpha, o)
```

```cpp
#include <hip/hip_runtime.h>
#include <hip/hip_bf16.h>
#include <stdint.h>

#define NN 10000
#define EE 320000
#define DD 128
typedef unsigned short u16;
__device__ __forceinline__ float bf2f(u16 v){ return __uint_as_float(((unsigned)v)<<16); }
__device__ __forceinline__ u16 f2bf(float f){ unsigned u=__float_as_uint(f); return (u16)((u+0x7FFF+((u>>16)&1))>>16); }

__global__ void k_zero(int* cnt){ int i=blockIdx.x*256+threadIdx.x; if(i<NN+1) cnt[i]=0; }
__global__ void k_hist(const int* dst,int* cnt){ int e=blockIdx.x*256+threadIdx.x; if(e<EE) atomicAdd(&cnt[dst[e]],1); }
__global__ void k_scan(const int* cnt,int* off,int* cur){
  __shared__ int s[1024]; int t=threadIdx.x; int loc[10]; int sum=0;
  for(int i=0;i<10;i++){ int n=t*10+i; int c=n<NN?cnt[n]:0; loc[i]=sum; sum+=c; }
  s[t]=sum; __syncthreads();
  for(int d=1;d<1024;d<<=1){ int v=t>=d?s[t-d]:0; __syncthreads(); s[t]+=v; __syncthreads(); }
  int base=s[t]-sum;
  for(int i=0;i<10;i++){ int n=t*10+i; if(n<NN){ off[n]=base+loc[i]; cur[n]=base+loc[i]; } }
  if(t==1023) off[NN]=s[t];
}
__global__ void k_fill(const int* src,const int* dst,int* cur,int* col){ int e=blockIdx.x*256+threadIdx.x; if(e<EE){ int p=atomicAdd(&cur[dst[e]],1); col[p]=src[e]; } }
__global__ void k_copy0(const u16* x,u16* tok){ int i=blockIdx.x*256+threadIdx.x; if(i<NN*DD){ int n=i>>7,d=i&127; tok[(n*8)*DD+d]=x[i]; } }
__global__ void k_spmm(const int* off,const int* col,const u16* dis,u16* tok,int k){
  int n=blockIdx.x*2+(threadIdx.x>>7); int d=threadIdx.x&127; if(n>=NN) return;
  float acc=0; for(int p=off[n];p<off[n+1];p++){ int s=col[p]; float g=bf2f(f2bf(bf2f(tok[(s*8+k-1)*DD+d])*bf2f(dis[s]))); acc+=g; }
  tok[(n*8+k)*DD+d]=f2bf(acc*bf2f(dis[n]));
}
__global__ void __launch_bounds__(128) k_tf(const u16* tok,const u16* wq,const u16* wk,const u16* wv,const u16* wo,const u16* wr,u16* out){
  __shared__ float T[8][128],X[8][128],Q[8][128],K[8][128],V[8][128],O[8][128],S[8][8],A[8];
  int n=blockIdx.x,d=threadIdx.x;
  for(int k=0;k<8;k++) X[k][d]=bf2f(tok[(n*8+k)*DD+d]);
  __syncthreads();
  _Pragma("unroll 1") for(int k=0;k<8;k++){ float m=0; for(int j=0;j<128;j++) m+=X[k][j]; m/=128; float v=0; for(int j=0;j<128;j++){ float z=X[k][j]-m; v+=z*z;} v/=128; T[k][d]=(X[k][d]-m)/sqrtf(v+1e-5f); }
  __syncthreads();
  _Pragma("unroll 1") for(int k=0;k<8;k++){ float q=0,kk=0,vv=0; for(int j=0;j<128;j++){ float t=T[k][j]; q+=t*bf2f(wq[j*128+d]); kk+=t*bf2f(wk[j*128+d]); vv+=t*bf2f(wv[j*128+d]); } Q[k][d]=q;K[k][d]=kk;V[k][d]=vv; }
  __syncthreads();
  if(d<64){ int a=d>>3,b=d&7; float s=0; for(int j=0;j<128;j++) s+=Q[a][j]*K[b][j]; S[a][b]=s*0.08838834764831845f; }
  __syncthreads();
  if(d<8){ float m=-1e30f; for(int b=0;b<8;b++) m=fmaxf(m,S[d][b]); float z=0; for(int b=0;b<8;b++){ S[d][b]=expf(S[d][b]-m); z+=S[d][b]; } for(int b=0;b<8;b++) S[d][b]/=z; }
  __syncthreads();
  for(int k=0;k<8;k++){ float o=0; for(int b=0;b<8;b++) o+=S[k][b]*V[b][d]; O[k][d]=o; }
  __syncthreads();
  _Pragma("unroll 1") for(int k=0;k<8;k++){ float o=0; for(int j=0;j<128;j++) o+=O[k][j]*bf2f(wo[j*128+d]); Q[k][d]=X[k][d]+o; }
  __syncthreads();
  if(d<8){ int k=d; float m=0; for(int j=0;j<128;j++) m+=Q[k][j]; m/=128; float v=0; for(int j=0;j<128;j++){ float z=Q[k][j]-m; v+=z*z;} v/=128; float r=1.0f/sqrtf(v+1e-5f); float s=0; for(int j=0;j<128;j++) s+=(Q[k][j]-m)*r*bf2f(wr[j]); A[k]=s; }
  __syncthreads();
  float m=-1e30f; for(int k=0;k<8;k++) m=fmaxf(m,A[k]); float z=0,o=0; for(int k=0;k<8;k++){ float e=expf(A[k]-m); z+=e; o+=e*Q[k][d]; }
  out[n*DD+d]=f2bf(o/z);
}
extern "C" void kernel_launch(void* const* d_in, const int* in_sizes, int n_in, void* d_out, int out_size, void* d_ws, size_t ws_size, hipStream_t stream){
  const u16* x=(const u16*)d_in[0]; const int* src=(const int*)d_in[1]; const int* dst=(const int*)d_in[2]; const u16* dis=(const u16*)d_in[3];
  const u16* wq=(const u16*)d_in[4]; const u16* wk=(const u16*)d_in[5]; const u16* wv=(const u16*)d_in[6]; const u16* wo=(const u16*)d_in[7]; const u16* wr=(const u16*)d_in[8];
  char* ws=(char*)d_ws; int* cnt=(int*)ws; int* off=(int*)(ws+65536); int* cur=(int*)(ws+131072); int* col=(int*)(ws+196608); u16* tok=(u16*)(ws+2097152);
  k_zero<<<(NN+256)/256,256,0,stream>>>(cnt);
  k_hist<<<EE/256,256,0,stream>>>(dst,cnt);
  k_scan<<<1,1024,0,stream>>>(cnt,off,cur);
  k_fill<<<EE/256,256,0,stream>>>(src,dst,cur,col);
  k_copy0<<<NN*DD/256,256,0,stream>>>(x,tok);
  for(int k=1;k<8;k++) k_spmm<<<NN/2,256,0,stream>>>(off,col,dis,tok,k);
  k_tf<<<NN,128,0,stream>>>(tok,wq,wk,wv,wo,wr,(u16*)d_out);
}
```

```cpp
#include <hip/hip_runtime.h>
#include <hip/hip_bf16.h>
#include <stdint.h>
#include <type_traits>

#define NN 10000
#define EE 320000
typedef unsigned short u16;
typedef unsigned u32;
typedef unsigned long long u64;
typedef u32 v4u __attribute__((ext_vector_type(4)));
typedef u32 v2u __attribute__((ext_vector_type(2)));
typedef short v4s __attribute__((ext_vector_type(4)));
typedef float v4f __attribute__((ext_vector_type(4)));
typedef __bf16 v4bf __attribute__((ext_vector_type(4)));
typedef __bf16 v8bf __attribute__((ext_vector_type(8)));
typedef __bf16 v16bf __attribute__((ext_vector_type(16)));
typedef __bf16 v2bf __attribute__((ext_vector_type(2)));
typedef float v2f __attribute__((ext_vector_type(2)));
typedef u16 v2h __attribute__((ext_vector_type(2)));
#define LAS __attribute__((address_space(3)))

#define GSTRIDE 81920u
#define GBUF    (32u * GSTRIDE)
#define WS_G    0u
#define WS_SUB  (8u * GBUF)
#define WS_ELL  (WS_SUB + 4096000u)
#define WS_CNT  (WS_ELL + 1280000u)
#define WS_OVF1 (WS_CNT + 256u)
#define WS_OVF2 (WS_OVF1 + 128u * 2504u * 4u)
#define WS_DBG  (WS_OVF2 + 1280000u)
#define POISON 0xFFFFFFFFu
#define OOB 0x40000000u
#define ZROW 15312u
#define MAXSPIN 100000u
#define AUX1 16

#define STAMP_INIT
#define STAMP(i)
#define STAMP_END

#define PSTAMP(i) STAMP(i)
__device__ __forceinline__ float bf2f(u16 v) { return __uint_as_float(((u32)v) << 16); }
__device__ __forceinline__ float bflo(u32 v) { return __uint_as_float(v << 16); }
__device__ __forceinline__ float bfhi(u32 v) { return __uint_as_float(v & 0xffff0000u); }
__device__ __forceinline__ u32 pk2(float a, float b) { v2f f = {a, b}; v2bf r = __builtin_convertvector(f, v2bf); return __builtin_bit_cast(u32, r); }
__device__ __forceinline__ u32 pk2s(float a, float b) { return min(pk2(a, b), 0xFFFFFFFEu); }
#define DPPU(v, ctrl) ((u32)__builtin_amdgcn_mov_dpp((int)(v), (ctrl), 0xF, 0xF, true))
#define LBAR() do { asm volatile("" ::: "memory"); __builtin_amdgcn_s_waitcnt(0xC07F); __builtin_amdgcn_s_barrier(); asm volatile("" ::: "memory"); } while (0)
__device__ __forceinline__ u32 tabidx(u32 n) {
  u32 s = (n * 52429u) >> 21, l = n - s * 40u;
  u32 row = l < 32u ? ((l >> 4) << 12) + 16u * s + (l & 15u) : 8192u - 32u + 8u * s + l;
  return row + (row >= 5120u ? 5120u : 0u);
}

__global__ void __launch_bounds__(640) k1(const int* __restrict__ src, const int* __restrict__ dst, const u16* __restrict__ x,
                                          const u16* __restrict__ dis, char* __restrict__ ws) {
#define KID 0
  __shared__ __attribute__((aligned(16))) u32 L[8704];
  const int t = threadIdx.x, b = blockIdx.x;
  const __amdgpu_buffer_rsrc_t rs = __builtin_amdgcn_make_buffer_rsrc(ws, 0, WS_DBG, 0x00020000);
  STAMP_INIT STAMP(0);
  if (b < 128) {
    u32* stage = L; u32* cnt = L + 8000; u32* novf = L + 8256;
    v4u s4 = {0, 0, 0, 0}, d4 = {0, 0, 0, 0};
    if (t < 625) { s4 = *(const v4u*)(src + b * 2500 + t * 4); d4 = *(const v4u*)(dst + b * 2500 + t * 4); }
    if (t < 257) cnt[t] = 0;
    if (b == 0 && t == 0) *(u32*)(ws + WS_CNT) = 0;
    __syncthreads();
    STAMP(1);
    u32* ovf = (u32*)(ws + WS_OVF1) + b * 2504;
    if (t < 625) {
      u32 r[4], bin[4], en[4];
#pragma unroll
      for (int i = 0; i < 4; i++) {
        u32 d = d4[i];
        bin[i] = (d * 52429u) >> 21;
        r[i] = atomicAdd(&cnt[bin[i]], 1u);
        en[i] = tabidx(s4[i]) | ((d - bin[i] * 40u) << 16);
      }
#pragma unroll
      for (int i = 0; i < 4; i++) {
        if (r[i] < 31) stage[bin[i] * 32 + 1 + r[i]] = en[i];
        else { u32 o = atomicAdd(novf, 1u); ovf[4 + o] = (en[i] & 0xffffu) | (d4[i] << 16); }
      }
    }
    __syncthreads();
    STAMP(2);
    if (t == 0) ovf[0] = *novf;
#pragma unroll
    for (int it = 0; it < 4; it++) {
      int i = t + it * 640;
      if (i < 2000) {
        int bin = i >> 3, q = i & 7;
        u32 c = cnt[bin];
        if ((u32)(q * 4) <= c) {
          v4u v = *(const v4u*)(stage + bin * 32 + q * 4);
          if (q == 0) v[0] = c;
          __builtin_amdgcn_raw_buffer_store_b128(v, rs, WS_SUB + b * 128 + bin * 16384 + q * 16, 0, AUX1);
        }
      }
    }
    STAMP(3);
  } else {
    const int wb = b - 128;
    char* S = (char*)L;
    v4u v[2]; float dv[2]; u32 go[2];
#pragma unroll
    for (int it = 0; it < 2; it++) {
      int i = t + it * 640; int node = i >> 4, cq = i & 15;
      v[it] = *(const v4u*)(x + (size_t)(wb * 80 + node) * 128 + cq * 8);
      dv[it] = bf2f(dis[wb * 80 + node]);
    }
#pragma unroll
    for (int it = 0; it < 2; it++) {
      int i = t + it * 640; int g = i / 40, pc = i - g * 40;
      go[it] = g * GSTRIDE + (pc < 32 ? (pc >> 4) * 32768 + wb * 256 + (pc & 15) * 16 : 65536 + wb * 128 + (pc - 32) * 16);
    }
#pragma unroll
    for (int it = 0; it < 2; it++) {
      int i = t + it * 640; int node = i >> 4, cq = i & 15;
      int s = node >= 40, l = node - s * 40;
      int pos = l < 32 ? (l >> 4) * 32 + s * 16 + (l & 15) : 32 + s * 8 + l;
      v2u a, c;
      a[0] = pk2s(bflo(v[it][0]) * dv[it], bfhi(v[it][0]) * dv[it]);
      a[1] = pk2s(bflo(v[it][1]) * dv[it], bfhi(v[it][1]) * dv[it]);
      c[0] = pk2s(bflo(v[it][2]) * dv[it], bfhi(v[it][2]) * dv[it]);
      c[1] = pk2s(bflo(v[it][3]) * dv[it], bfhi(v[it][3]) * dv[it]);
      *(v2u*)(S + (2 * cq) * 656 + pos * 8) = a;
      *(v2u*)(S + (2 * cq + 1) * 656 + pos * 8) = c;
    }
    __syncthreads();
    STAMP(2);
#pragma unroll
    for (int it = 0; it < 2; it++) {
      int i = t + it * 640; int g = i / 40, pc = i - g * 40;
      v4u o = *(const v4u*)(S + g * 656 + pc * 16);
      __builtin_amdgcn_raw_buffer_store_b128(o, rs, WS_G + go[it], 0, AUX1);
    }
    const v4u pz = {POISON, POISON, POISON, POISON};
#pragma unroll
    for (int it = 0; it < 4; it++) __builtin_amdgcn_raw_buffer_store_b128(pz, rs, WS_G + go[it & 1], (1 + (it >> 1)) * GBUF, 16);
    STAMP(3);
  }
  STAMP_END;
#undef KID
}

#define B_ELL 0
#define B_CB 1280
#define B_CN 1472
#define B_BUSY 2240
#define B_FULL 2432
#define B_ROWM 2480
#define B_NT 2576
#define B_NF 2577
#define B_Z0 1280
#define B_ZN 1300
#define B_Q 2592
#define B_F 6752
#define B_END 10848
__global__ void __launch_bounds__(1024) k1b(char* __restrict__ ws) {
#define KID 1
  __shared__ __attribute__((aligned(16))) u32 L[B_END];
  const int t = threadIdx.x, slot = blockIdx.x, lane = t & 63, w = __builtin_amdgcn_readfirstlane(t >> 6);
  STAMP_INIT STAMP(0);
  const v4u v = *(const v4u*)(ws + WS_SUB + min(slot, 249) * 16384 + t * 16);
  u32 n1 = 0;
  if (slot < 128) n1 = ((const u32*)(ws + WS_OVF1))[slot * 2504];
  if (t < 320) { const v4u z = {POISON, POISON, POISON, POISON}; *(v4u*)(L + B_ELL + t * 4) = z; }
  else if (t < 320 + B_ZN / 4) { const v4u z = {0, 0, 0, 0}; *(v4u*)(L + B_Z0 + (t - 320) * 4) = z; }
  LBAR();
  STAMP(1);
  u32* const gcnt = (u32*)(ws + WS_CNT);
  u32* const ovf2 = (u32*)(ws + WS_OVF2);
  {
    const u32 q = t & 7;
    u32 c = DPPU(v[0], 0x00);
    c = (u32)__builtin_amdgcn_update_dpp((int)c, (int)c, 0x114, 0xF, 0xA, false);
    c = min(c, 31u);
    u32 inc = c + (u32)__builtin_amdgcn_update_dpp(0, (int)c, 0x118, 0xF, 0xC, false);
    inc += (u32)__builtin_amdgcn_update_dpp(0, (int)inc, 0x142, 0xA, 0xF, false);
    inc += (u32)__builtin_amdgcn_update_dpp(0, (int)inc, 0x143, 0xC, 0xF, false);
    u32 base = 0;
    if (lane == 63) base = atomicAdd(&L[B_NT], inc);
    base = __builtin_amdgcn_readlane(base, 63);
    const u32 pos = B_Q + base + inc - c + 4 * q - 1;
#pragma unroll
    for (int i = 0; i < 4; i++) {
      const u32 id = 4 * q + i;
      L[(id - 1u < c) ? pos + i : B_Q + 4096 + lane] = v[i];
    }
  }
  if (n1) {
    const u32* o1 = (const u32*)(ws + WS_OVF1) + slot * 2504;
    for (u32 i = t; i < n1; i += 1024) ovf2[atomicAdd(gcnt, 1u)] = o1[4 + i];
  }
  LBAR();
  STAMP(2);
  if (w >= 12 || slot >= 250) {
    const __amdgpu_buffer_rsrc_t rp = __builtin_amdgcn_make_buffer_rsrc(ws + WS_G + 3 * GBUF + slot * 40960, 0, 40960, 0x00020000);
    const v4u pz = {POISON, POISON, POISON, POISON};
    if (w >= 12) {
#pragma unroll
      for (int i = 0; i < 10; i++) __builtin_amdgcn_raw_buffer_store_b128(pz, rp, (t - 768) * 16 + i * 4096, 0, 16);
    }
    return;
  }
  auto place = [&](u32 e, u32 r, u32 c) {
    const u32 loc = e >> 16, hp = loc >> 3;
    const u32 j = hp == 4 ? 24 + (r >> 1) : (r < 12 ? (hp >> 1) * 12 + r : 20 + (hp >> 1) * 4 + r);
    const u32 half = (hp == 4 ? r : hp) & 1;
    const u32 ln = half * 32 + (loc & 4) * 4 + c * 4 + (loc & 3);
    ((u16*)(L + B_ELL))[(j >> 3) * 512 + ln * 8 + (j & 7)] = (u16)e;
    atomicOr(&L[B_ROWM + half * 40 + j], 1u << ((e + 16) & 31));
  };
  const u32 tot = L[B_NT];
  for (u32 b0 = w * 128; b0 < tot; b0 += 1536) {
    u32 e[2], hb[2], rk[2], r[2], c[2];
#pragma unroll
    for (int i = 0; i < 2; i++) { const u32 id = b0 + i * 64 + lane; e[i] = L[B_Q + id]; if (id >= tot) e[i] = ((40 + (lane & 7)) << 16) | (lane & 31); }
#pragma unroll
    for (int i = 0; i < 2; i++) { hb[i] = (e[i] >> 19) * 32 + (e[i] & 31); rk[i] = atomicAdd(&L[B_CB + hb[i]], 1u); }
#pragma unroll
    for (int i = 0; i < 2; i++) {
      r[i] = e[i] >= (32u << 16) ? (rk[i] + (e[i] >> 1)) & 15 : (rk[i] < 12 ? rk[i] : rk[i] - 12);
      c[i] = atomicAdd(&L[rk[i] < 24 ? B_CN + (e[i] >> 16) * 16 + r[i] : B_CN + 640 + lane], 1u);
    }
#pragma unroll
    for (int i = 0; i < 2; i++) {
      if (e[i] < (40u << 16)) {
        if (rk[i] < 24 && c[i] < 4) {
          place(e[i], r[i], c[i]);
          atomicOr(&L[B_BUSY + hb[i]], 1u << r[i]);
          if (c[i] == 3) atomicOr(&L[B_FULL + (e[i] >> 16)], 1u << r[i]);
        } else L[B_F + atomicAdd(&L[B_NF], 1u)] = e[i];
      }
    }
  }
  LBAR();
  STAMP(3);
  const u32 nf = L[B_NF];
  for (u32 id = t; id < nf; id += 768) {
    const u32 e = L[B_F + id], loc = e >> 16, hb = (e >> 19) * 32 + (e & 31);
    u32 rho = (id * 5 + (id >> 2)) & 15, tried = 0;
    const u32 pm = loc >= 32 ? 0xFFFFu : 0xFFFu;
    for (int round = 0;; round++) {
      const u32 bs = __hip_atomic_load(&L[B_BUSY + hb], __ATOMIC_RELAXED, __HIP_MEMORY_SCOPE_WORKGROUP);
      const u32 nfl = ~(__hip_atomic_load(&L[B_FULL + loc], __ATOMIC_RELAXED, __HIP_MEMORY_SCOPE_WORKGROUP) | tried);
      u32 cand = nfl & ~bs & pm;
      const bool strict = cand != 0 && round < 2;
      if (!cand) cand = nfl & pm;
      if (!cand) cand = nfl & ~bs & 0xF000;
      if (!cand) cand = nfl & 0xF000;
      if (!cand) { ovf2[atomicAdd(gcnt, 1u)] = (e & 0xffffu) | ((slot * 40 + loc) << 16); break; }
      const u32 r = (rho + __builtin_ctz((cand | (cand << 16)) >> rho)) & 15;
      const u32 ob = atomicOr(&L[B_BUSY + hb], 1u << r);
      const u32 c = atomicAdd(&L[B_CN + loc * 16 + r], 1u);
      if (c == 3) atomicOr(&L[B_FULL + loc], 1u << r);
      if (c < 4 && !(strict && ((ob >> r) & 1))) { place(e, r, c); break; }
      if (c >= 3) tried |= 1u << r;
      rho = (rho + 7) & 15;
    }
  }
  LBAR();
  STAMP(4);
  if (t < 320) {
    v4u o = *(const v4u*)(L + B_ELL + t * 4);
    const u32* rm = L + B_ROWM + (lane >> 5) * 40 + w * 8;
    const v4u m0 = *(const v4u*)rm, m1 = *(const v4u*)(rm + 4);
#pragma unroll
    for (int i = 0; i < 4; i++) {
      const u32 a = i < 2 ? m0[2 * i] : m1[2 * i - 4], bb = i < 2 ? m0[2 * i + 1] : m1[2 * i - 3];
      u32 fa, fb;
      asm("v_ffbl_b32 %0, %1" : "=v"(fa) : "v"(~a)); asm("v_ffbl_b32 %0, %1" : "=v"(fb) : "v"(~bb));
      v2h d = {(u16)(fa + ZROW), (u16)(fb + ZROW)};
      const u32 oi = o[i];
      o[i] = __builtin_bit_cast(u32, __builtin_elementwise_min(__builtin_bit_cast(v2h, oi), d));
    }
    const __amdgpu_buffer_rsrc_t rs = __builtin_amdgcn_make_buffer_rsrc(ws, 0, WS_DBG, 0x00020000);
    __builtin_amdgcn_raw_buffer_store_b128(o, rs, WS_ELL + slot * 5120 + t * 16, 0, AUX1);
  }
  STAMP(5);
  STAMP_END;
#undef KID
}

__device__ __forceinline__ v4bf rd(u32 a) { return __builtin_amdgcn_ds_read_tr16_b64_v4bf16((LAS v4bf*)(size_t)a); }
__device__ __forceinline__ v16bf rd4(u32 a0, u32 a1, u32 a2, u32 a3) {
  const v4bf a = rd(a0), b = rd(a1), c = rd(a2), d = rd(a3);
  return __builtin_shufflevector(__builtin_shufflevector(a, b, 0, 1, 2, 3, 4, 5, 6, 7), __builtin_shufflevector(c, d, 0, 1, 2, 3, 4, 5, 6, 7),
                                 0, 1, 2, 3, 4, 5, 6, 7, 8, 9, 10, 11, 12, 13, 14, 15);
}
#define DMA(voff, soff, lbase) asm volatile("s_mov_b32 m0, %2\n\ts_nop 0\n\tbuffer_load_dwordx4 %0, %3, %1 offen sc1 lds" :: "v"(voff), "s"(soff), "s"(lbase), "s"(srd) : "memory")
#define PFM 1
#define PR1 0
#define PR2 2
#define PR3 4
#define NPRE 1
#define KMAX 7
#define LASTP 6
#define D23POS 0
#define D4POS 2
#define T2PRE
__global__ void __launch_bounds__(1024) k2(const u16* __restrict__ dis, char* __restrict__ ws) {
#define KID 2
  __shared__ __attribute__((aligned(16))) char T[163840];
  const int t = threadIdx.x, lane = t & 63, w = __builtin_amdgcn_readfirstlane(t >> 6);
  const int b = blockIdx.x, gp = (b & 7) * 2 + (b >> 7), p = (b >> 3) & 15;
  const int slot = p * 16 + w;
  const bool act = slot < 250;
  asm volatile("" :: "v"((u32)(size_t)(LAS char*)T));
  STAMP_INIT STAMP(0);
  const __amdgpu_buffer_rsrc_t rs = __builtin_amdgcn_make_buffer_rsrc(ws + WS_G, 0, 8u * GBUF, 0x00020000);
  const v4u srd = {(u32)(size_t)(ws + WS_G), (u32)((size_t)(ws + WS_G) >> 32) & 0xffffu, 8u * GBUF, 0x00020000u};
  const u32 sgA = gp * 2 * GSTRIDE;
  const u32 vla = w * 1024 + lane * 16;
  const u32 vlc = (w == 15 && lane >= 40) ? OOB : vla;
  const u32 lb0 = w * 1024, lb2 = w * 1024 + 32768 + (w >= 8 ? 40960 : 0);
  DMA(vla, sgA, lb0); DMA(vla, sgA + 16384, lb0 + 16384); DMA(vla, sgA + 32768, lb2); DMA(vla, sgA + 49152, lb0 + 90112); DMA(vlc, sgA + 65536, lb0 + 106496);
  v4u e[5]; u16 dvr[3];
  const int sl = act ? slot : 0;
#pragma unroll
  for (int i = 0; i < 5; i++) e[i] = *(const v4u*)(ws + WS_ELL + sl * 5120 + i * 1024 + lane * 16);
  dvr[0] = dis[sl * 40 + (lane >> 2)]; dvr[1] = dis[sl * 40 + 16 + (lane >> 2)]; dvr[2] = dis[sl * 40 + 32 + ((lane & 31) >> 2)];
  const u32 novf = *(const u32*)(ws + WS_CNT);
  const u32 vp01 = act ? slot * 128 + (lane >> 1) * 4 : OOB;
  const u32 vp2 = act ? 65536 + slot * 64 + (lane >> 1) * 4 : OOB;
  v8bf sel1, sel2; const int seli = (lane & 1) ? 0xEEEEEEEE : 0x44444444;
  {
    const u32 on = (lane & 2) ? 0u : 0x3f803f80u, lo = (lane & 4) ? 0u : on, hi = (lane & 4) ? on : 0u;
    const bool m1 = ((lane >> 4) & 1) == ((lane >> 3) & 1);
    const v4u a2 = {lo, lo, hi, hi}, a1 = {m1 ? lo : 0u, m1 ? lo : 0u, m1 ? hi : 0u, m1 ? hi : 0u};
    sel1 = __builtin_bit_cast(v8bf, a1); sel2 = __builtin_bit_cast(v8bf, a2);
  }
  u32 vchk;
  {
    const u32 i = (lane >> 3) & 3, ln = (w == 15 && (i & 1)) ? (lane & 1) : (lane & 7);
    const u32 hl = (w == 15 && (lane & 15) >= 10) ? (lane & 7) : (lane & 15);
    vchk = lane < 32 ? (i == 2 ? lb2 : lb0 + (i == 1 ? 16384 : (i == 3 ? 90112 : 0))) + ln * 128 + lane * 4 : lb0 + 106496 + hl * 64 + (lane & 15) * 4;
  }
  if (t < 32) { const v4u z = {0, 0, 0, 0}; *(LAS v4u*)(size_t)(ZROW * 8 + (t >> 4) * 40960 + (t & 15) * 16) = z; }
  u32 ad[32];
#pragma unroll
  for (int i = 0; i < 16; i++) { const u32 q = e[i >> 2][i & 3]; ad[2 * i] = (q & 0xffffu) * 8; ad[2 * i + 1] = (q >> 16) * 8; }
  u32 ex[4] = {e[4][0], e[4][1], e[4][2], e[4][3]};
  bool xm;
  {
    const u32 m0 = min(min(ex[0] & 0xffffu, ex[0] >> 16), min(ex[1] & 0xffffu, ex[1] >> 16));
    const u32 m1 = min(min(ex[2] & 0xffffu, ex[2] >> 16), min(ex[3] & 0xffffu, ex[3] >> 16));
    xm = __builtin_amdgcn_ballot_w64(min(m0, m1) < ZROW) != 0;
  }
  float dv2[3];
#pragma unroll
  for (int i = 0; i < 3; i++) { const float d = bf2f(dvr[i]); dv2[i] = d * d; }
  asm volatile("" ::: "memory");
  __builtin_amdgcn_s_waitcnt(0x0070);
  __builtin_amdgcn_s_barrier();
  asm volatile("" ::: "memory");
  STAMP(1);

  auto slow = [&](const u32 tofs, const u32 n0, const u32 nn) __attribute__((always_inline)) -> float {
    const u32* ov = (const u32*)(ws + WS_OVF2);
    float a = 0.f;
    for (u32 base = 0; base < novf; base += 64) {
      const u32 en = base + lane < novf ? ov[base + lane] : POISON;
      u64 m = __builtin_amdgcn_ballot_w64((en >> 16) - n0 < nn);
      while (m) {
        const int l = __builtin_ctzll(m); m &= m - 1;
        const u32 es = __builtin_amdgcn_readlane(en, l);
        const float val = bf2f(*(LAS u16*)(size_t)((es & 0xffffu) * 8 + tofs + (lane & 3) * 2));
        a += ((es >> 16) - n0 == (u32)(lane >> 2)) ? val : 0.f;
      }
    }
    return a;
  };
  auto grow = [&](const u32 la, const u32 scur) __attribute__((always_inline)) -> v2u {
    v2u v = {0, 0};
    if (la < ZROW * 8) {
      u32 spins = 0;
      do {
        v = __builtin_amdgcn_raw_buffer_load_b64(rs, la >= 81920 ? la - 40960 : la, scur, 16);
        asm volatile("" ::: "memory");
      } while (max(v[0], v[1]) == POISON && ++spins < MAXSPIN);
    }
    return v;
  };
  auto fallback = [&](const int bt, const u32 scur) __attribute__((always_inline)) -> float {
    float s0 = 0.f, s1 = 0.f, s2 = 0.f, s3 = 0.f;
#pragma unroll 1
    for (int j = 0; j < (bt < 2 ? 16 : 8); j++) {
      const int je = j < 12 ? bt * 12 + j : 20 + bt * 4 + j;
      const u32 la = *(const u16*)(ws + WS_ELL + sl * 5120 + (je >> 3) * 1024 + lane * 16 + (je & 7) * 2) * 8u;
      const v2u v = grow(la, scur);
      s0 += bflo(v[0]); s1 += bfhi(v[0]); s2 += bflo(v[1]); s3 += bfhi(v[1]);
    }
    float a = 0.f;
#pragma unroll
    for (int c = 0; c < 4; c++) {
      const int sl_ = (lane & 48) + 4 * c + ((lane >> 2) & 3);
      const float t0 = __shfl(s0, sl_), t1 = __shfl(s1, sl_), t2 = __shfl(s2, sl_), t3 = __shfl(s3, sl_);
      a += (lane & 2) ? ((lane & 1) ? t3 : t2) : ((lane & 1) ? t1 : t0);
    }
    if (bt == 2) a += __shfl_xor(a, 32);
    const u32* ov = (const u32*)(ws + WS_OVF2);
    const u32 n0 = slot * 40 + bt * 16;
    for (u32 i = 0; i < novf; i++) {
      const u32 es = ov[i];
      if ((es >> 16) - n0 == (u32)(lane >> 2) && (bt < 2 || lane < 32)) {
        const v2u v = grow((es & 0xffffu) * 8, scur);
        const u32 q = (lane & 2) ? v[1] : v[0];
        a += (lane & 1) ? bfhi(q) : bflo(q);
      }
    }
    return a;
  };
  auto tail = [&](const int bt, const v4f acc, const u32 scur, const u32 voff, const u32 soff) __attribute__((always_inline)) {
    float a = acc[0] + acc[1];
    if (__builtin_expect(__builtin_amdgcn_ballot_w64(a != a) != 0, 0)) a = fallback(bt, scur);
    const float d2 = dv2[bt];
    const float tn = __builtin_bit_cast(float, DPPU(__float_as_uint(a), 0xB1)) * d2;
    const u32 cm = min(pk2(a * d2, tn), 0xFFFFFFFEu);
    asm volatile("s_mov_b64 exec, %4\n\tbuffer_store_dword %0, %1, %2, %3 offen sc1\n\ts_mov_b64 exec, -1"
                 :: "v"(cm), "v"(voff), "s"(srd), "s"(soff), "s"(bt < 2 ? 0x5555555555555555ull : 0x55555555ull) : "memory");
  };
  v4f acc = {0, 0, 0, 0}, acc2 = {0, 0, 0, 0};
  u32 vp2x = OOB;
  auto phase = [&](auto GBc, auto XMc, const bool recv, const u32 sprev, const u32 scur, const u32 srecv_, const int sb) __attribute__((always_inline)) {
    const u32 srecv = srecv_;
    constexpr u32 GB = decltype(GBc)::value;
    constexpr bool XM = decltype(XMc)::value;
    constexpr u32 to = GB * 40960u, ro = (1 - GB) * 40960u;
    constexpr int NQ = XM ? 10 : 8, E0 = XM ? 3 : 2, E1 = XM ? 7 : 5;
    __builtin_amdgcn_s_setprio(3);
    if (recv) { DMA(vla, srecv, lb0 + ro); DMA(vla, srecv + 16384, lb0 + 16384 + ro); }
    auto grp = [&](const int s) __attribute__((always_inline)) -> v16bf {
      if (XM && (s == E0 || s == E1)) {
        u32 q0 = ex[s == E0 ? 0 : 2], q1 = ex[s == E0 ? 1 : 3];
        asm volatile("" : "+v"(q0), "+v"(q1));
        return rd4((q0 & 0xffffu) * 8 + to, (q0 >> 16) * 8 + to, (q1 & 0xffffu) * 8 + to, (q1 >> 16) * 8 + to);
      }
      const int q = XM ? s - (s > E0) - (s > E1) : s;
      return rd4(ad[4 * q] + to, ad[4 * q + 1] + to, ad[4 * q + 2] + to, ad[4 * q + 3] + to);
    };
    v16bf g[3];
#pragma unroll
    for (int q = 0; q < 3; q++) g[q] = grp(q);
    __builtin_amdgcn_sched_barrier(0);
    if (D23POS < 0 && recv) { DMA(vla, srecv + 32768, lb2 + ro); DMA(vla, srecv + 49152, lb0 + 90112 + ro); }
    float e0 = 0.f, e1 = 0.f, e2 = 0.f;
    if (__builtin_expect(novf != 0, 0)) { e0 = slow(to, slot * 40, 16); e1 = slow(to, slot * 40 + 16, 16); e2 = slow(to, slot * 40 + 32, 8); }
    __builtin_amdgcn_sched_barrier(0);
    u32 chk = 0;
#pragma unroll
    for (int q = 0; q < NQ; q++) {
      if (q == 0) { acc[0] = e0; acc[1] = 0.f; }
      if (q == E0 + 1) { acc[0] = e1; acc[1] = 0.f; }
      if (q == E1 + 1) { acc2[0] = e2; acc2[1] = 0.f; }
      if (q == NQ - 1 && recv) {
        asm volatile("s_waitcnt vmcnt(%0)" :: "n"(D4POS <= E0 ? 2 : 1) : "memory");
        chk = *(volatile LAS u32*)(size_t)(vchk + ro);
      }
      if (q <= E1) acc = __builtin_amdgcn_smfmac_f32_16x16x64_bf16(sel1, g[q % 3], acc, seli, 0, 0);
      else acc2 = __builtin_amdgcn_smfmac_f32_16x16x64_bf16(sel2, g[q % 3], acc2, seli, 0, 0);
      if (q + 3 < NQ) g[q % 3] = grp(q + 3);
      if (q == D23POS && recv) { DMA(vla, srecv + 32768, lb2 + ro); DMA(vla, srecv + 49152, lb0 + 90112 + ro); }
      if (q == D4POS && recv) DMA(vlc, srecv + 65536, lb0 + 106496 + ro);
      if (q == PR1) __builtin_amdgcn_s_setprio(2);
      if (q == PR2) __builtin_amdgcn_s_setprio(1);
      if (q == PR3) __builtin_amdgcn_s_setprio(0);
      __builtin_amdgcn_sched_barrier(0);
      if (q == E0 || q == E1) {
        tail(q == E1, acc, scur, vp01, scur + GBUF + (q == E1 ? 32768 : 0));
        __builtin_amdgcn_sched_barrier(0);
      }
    }
    tail(2, acc2, scur, vp2, scur + GBUF);
    PSTAMP(sb);
    if (recv) {
      u32 spins = 0;
      if (__builtin_expect(__builtin_amdgcn_ballot_w64(chk == POISON) != 0, 0)) {
        do {
          spins++;
          DMA(vla, srecv, lb0 + ro); DMA(vla, srecv + 16384, lb0 + 16384 + ro);
          DMA(vla, srecv + 32768, lb2 + ro); DMA(vla, srecv + 49152, lb0 + 90112 + ro);
          DMA(vlc, srecv + 65536, lb0 + 106496 + ro);
          asm volatile("s_waitcnt vmcnt(0)" ::: "memory");
        } while (__builtin_amdgcn_ballot_w64(*(volatile LAS u32*)(size_t)(vchk + ro) == POISON) != 0 && spins < MAXSPIN);
      }
      PSTAMP(sb + 1);
      asm volatile("" ::: "memory");
      __builtin_amdgcn_s_barrier();
      asm volatile("" ::: "memory");
      PSTAMP(sb + 2);
    }
  };
  using F0 = std::integral_constant<u32, 0>; using F1 = std::integral_constant<u32, 1>;
  auto hops = [&](auto XMc) __attribute__((always_inline)) {
    u32 sk = sgA;
#pragma unroll 1
    for (int k = 1; k <= KMAX; k++) {
      phase(F0{}, XMc, true, sk - GBUF + GSTRIDE, sk, sk + GSTRIDE, 6 * k - 4);
      phase(F1{}, XMc, k < KMAX, sk, sk + GSTRIDE, sk + GBUF, 6 * k - 1);
      sk += GBUF;
    }
  };
  if (xm) hops(std::true_type{}); else hops(std::false_type{});
  STAMP(47);
  STAMP_END;
#undef KID
}

__device__ __forceinline__ u16 f2bf(float f) { return (u16)pk2(f, 0.f); }
__device__ __host__ __forceinline__ u32 rowof(u32 n) { u32 s = n / 40, l = n - s * 40; return l < 32 ? (l >> 4) * 4096 + 16 * s + (l & 15) : 8192 - 32 + 8 * s + l; }
__global__ void __launch_bounds__(128) k3(const u16* x, const u16* dis, const char* ws, const u16* wq, const u16* wk, const u16* wv, const u16* wo, const u16* wr, u16* out) {
  __shared__ float T[8][128], X[8][128], Q[8][128], K[8][128], V[8][128], O[8][128], S[8][8], A[8];
  int n = blockIdx.x, d = threadIdx.x;
  X[0][d] = bf2f(x[n * 128 + d]);
  float inv = bf2f(dis[n]); inv = inv == 0.f ? 1.f : 1.0f / inv;
  for (int k = 1; k < 8; k++) X[k][d] = inv * bf2f(*(const u16*)(ws + WS_G + (size_t)k * GBUF + (d >> 2) * GSTRIDE + rowof(n) * 8 + (d & 3) * 2));
  __syncthreads();
  _Pragma("unroll 1") for (int k = 0; k < 8; k++) { float m = 0; for (int j = 0; j < 128; j++) m += X[k][j]; m /= 128; float v = 0; for (int j = 0; j < 128; j++) { float z = X[k][j] - m; v += z * z; } v /= 128; T[k][d] = (X[k][d] - m) / sqrtf(v + 1e-5f); }
  __syncthreads();
  _Pragma("unroll 1") for (int k = 0; k < 8; k++) { float q = 0, kk = 0, vv = 0; for (int j = 0; j < 128; j++) { float t = T[k][j]; q += t * bf2f(wq[j * 128 + d]); kk += t * bf2f(wk[j * 128 + d]); vv += t * bf2f(wv[j * 128 + d]); } Q[k][d] = q; K[k][d] = kk; V[k][d] = vv; }
  __syncthreads();
  if (d < 64) { int a = d >> 3, b = d & 7; float s = 0; for (int j = 0; j < 128; j++) s += Q[a][j] * K[b][j]; S[a][b] = s * 0.08838834764831845f; }
  __syncthreads();
  if (d < 8) { float m = -1e30f; for (int b = 0; b < 8; b++) m = fmaxf(m, S[d][b]); float z = 0; for (int b = 0; b < 8; b++) { S[d][b] = expf(S[d][b] - m); z += S[d][b]; } for (int b = 0; b < 8; b++) S[d][b] /= z; }
  __syncthreads();
  for (int k = 0; k < 8; k++) { float o = 0; for (int b = 0; b < 8; b++) o += S[k][b] * V[b][d]; O[k][d] = o; }
  __syncthreads();
  _Pragma("unroll 1") for (int k = 0; k < 8; k++) { float o = 0; for (int j = 0; j < 128; j++) o += O[k][j] * bf2f(wo[j * 128 + d]); Q[k][d] = X[k][d] + o; }
  __syncthreads();
  if (d < 8) { int k = d; float m = 0; for (int j = 0; j < 128; j++) m += Q[k][j]; m /= 128; float v = 0; for (int j = 0; j < 128; j++) { float z = Q[k][j] - m; v += z * z; } v /= 128; float r = 1.0f / sqrtf(v + 1e-5f); float s = 0; for (int j = 0; j < 128; j++) s += (Q[k][j] - m) * r * bf2f(wr[j]); A[k] = s; }
  __syncthreads();
  float m = -1e30f; for (int k = 0; k < 8; k++) m = fmaxf(m, A[k]); float z = 0, o = 0; for (int k = 0; k < 8; k++) { float e = expf(A[k] - m); z += e; o += e * Q[k][d]; }
  out[n * 128 + d] = f2bf(o / z);
}

extern "C" void kernel_launch(void* const* d_in, const int* in_sizes, int n_in, void* d_out, int out_size, void* d_ws, size_t ws_size, hipStream_t stream) {
  const u16* x = (const u16*)d_in[0]; const int* src = (const int*)d_in[1]; const int* dst = (const int*)d_in[2]; const u16* dis = (const u16*)d_in[3];
  const u16* wq = (const u16*)d_in[4]; const u16* wk = (const u16*)d_in[5]; const u16* wv = (const u16*)d_in[6]; const u16* wo = (const u16*)d_in[7]; const u16* wr = (const u16*)d_in[8];
  char* ws = (char*)d_ws;
  k1<<<253, 640, 0, stream>>>(src, dst, x, dis, ws);
  k1b<<<256, 1024, 0, stream>>>(ws);
  k2<<<256, 1024, 0, stream>>>(dis, ws);
  k3<<<NN, 128, 0, stream>>>(x, dis, ws, wq, wk, wv, wo, wr, (u16*)d_out);
}
#define WS_TOK WS_G
#define TOKBYTES (8u * GBUF)
#define TOKSC(n) hbf2f(dis[n])
#define TOKADDR(n,k,d) ((size_t)(k) * GBUF + ((d) >> 2) * GSTRIDE + rowof(n) * 8 + ((d) & 3) * 2)
```

```cpp
#include <hip/hip_runtime.h>
#include <hip/hip_bf16.h>
#include <stdint.h>
#include <type_traits>

#define NN 10000
#define EE 320000
typedef unsigned short u16;
typedef unsigned u32;
typedef unsigned long long u64;
typedef u32 v4u __attribute__((ext_vector_type(4)));
typedef u32 v2u __attribute__((ext_vector_type(2)));
typedef short v4s __attribute__((ext_vector_type(4)));
typedef float v4f __attribute__((ext_vector_type(4)));
typedef __bf16 v4bf __attribute__((ext_vector_type(4)));
typedef __bf16 v8bf __attribute__((ext_vector_type(8)));
typedef __bf16 v16bf __attribute__((ext_vector_type(16)));
typedef __bf16 v2bf __attribute__((ext_vector_type(2)));
typedef float v2f __attribute__((ext_vector_type(2)));
typedef u16 v2h __attribute__((ext_vector_type(2)));
#define LAS __attribute__((address_space(3)))

#define GSTRIDE 81920u
#define GBUF    (32u * GSTRIDE)
#define WS_G    0u
#define WS_SUB  (8u * GBUF)
#define WS_ELL  (WS_SUB + 4096000u)
#define WS_CNT  (WS_ELL + 1280000u)
#define WS_OVF1 (WS_CNT + 256u)
#define WS_OVF2 (WS_OVF1 + 128u * 2504u * 4u)
#define WS_W    (WS_OVF2 + 1280000u)
#define WS_DBG  (WS_W + 69632u)
#define POISON 0xFFFFFFFFu
#define OOB 0x40000000u
#define ZROW 15312u
#define MAXSPIN 100000u
#define AUX1 16

#define STAMP_INIT
#define STAMP(i)
#define STAMP_END

#define WST
#define WSA
#define PSTAMP(i) STAMP(i)
__device__ __forceinline__ float bf2f(u16 v) { return __uint_as_float(((u32)v) << 16); }
__device__ __forceinline__ float bflo(u32 v) { return __uint_as_float(v << 16); }
__device__ __forceinline__ float bfhi(u32 v) { return __uint_as_float(v & 0xffff0000u); }
__device__ __forceinline__ u32 pk2(float a, float b) { v2f f = {a, b}; v2bf r = __builtin_convertvector(f, v2bf); return __builtin_bit_cast(u32, r); }
__device__ __forceinline__ u32 pk2s(float a, float b) { return min(pk2(a, b), 0xFFFFFFFEu); }
#define DPPU(v, ctrl) ((u32)__builtin_amdgcn_mov_dpp((int)(v), (ctrl), 0xF, 0xF, true))
#define LBAR() do { asm volatile("" ::: "memory"); __builtin_amdgcn_s_waitcnt(0xC07F); __builtin_amdgcn_s_barrier(); asm volatile("" ::: "memory"); } while (0)
__device__ __forceinline__ u32 tabidx(u32 n) {
  u32 s = (n * 52429u) >> 21, l = n - s * 40u;
  u32 row = l < 32u ? ((l >> 4) << 12) + 16u * s + (l & 15u) : 8192u - 32u + 8u * s + l;
  return row + (row >= 5120u ? 5120u : 0u);
}

#define DMA0(voff, soff, lbase) asm volatile("s_mov_b32 m0, %2\n\ts_nop 0\n\tbuffer_load_dwordx4 %0, %3, %1 offen lds" :: "v"(voff), "s"(soff), "s"(lbase), "s"(srd) : "memory")
__device__ __forceinline__ v4bf rd(u32 a) { return __builtin_amdgcn_ds_read_tr16_b64_v4bf16((LAS v4bf*)(size_t)a); }
#define MFMA(a, b, c) __builtin_amdgcn_mfma_f32_16x16x32_bf16(__builtin_bit_cast(v8bf, (v4u)(a)), __builtin_bit_cast(v8bf, (v4u)(b)), (c), 0, 0, 0)
__device__ __forceinline__ void sw16(float& a, float& b) { const v2u r = __builtin_amdgcn_permlane16_swap(__float_as_uint(a), __float_as_uint(b), false, false); a = __uint_as_float(r[0]); b = __uint_as_float(r[1]); }
__device__ __forceinline__ void sw32(float& a, float& b) { const v2u r = __builtin_amdgcn_permlane32_swap(__float_as_uint(a), __float_as_uint(b), false, false); a = __uint_as_float(r[0]); b = __uint_as_float(r[1]); }
#define DPPF(v, ctrl) __uint_as_float(DPPU(__float_as_uint(v), ctrl))
#define LOG2E 1.4426950408889634f
__device__ __forceinline__ void wprep(const int which, const u16* wq, const u16* wk, const u16* wv, const u16* wo, const u16* wr, char* ws, float* L WST) {
  const int t = threadIdx.x, lane = t & 63, m = __builtin_amdgcn_readfirstlane(t >> 6), g = lane >> 4, i = lane & 15;
  v4f D[8];
  v4u B[4];
  const float wra = bf2f(wr[lane]), wrb = bf2f(wr[64 + lane]), wrm = bf2f(wr[16 * (m & 7) + i]);
  const u32 lb = (u32)(size_t)(LAS float*)L;
  if (m < 8) {
    const u16* wa = which == 0 ? wq : wv;
    {
      const v4u srd = {(u32)(size_t)wa, (u32)((size_t)wa >> 32) & 0xffffu, 32768u, 0x00020000u};
#pragma unroll
      for (int ks = 0; ks < 4; ks++) DMA0(i * 256 + g * 16, m * 4096 + ks * 64, lb + (m * 4 + ks) * 1024);
    }
    if (which == 0) {
#pragma unroll
      for (int ks = 0; ks < 4; ks++) B[ks] = *(const v4u*)(wk + (16 * m + i) * 128 + 32 * ks + 8 * g);
    } else {
      const v4u srd = {(u32)(size_t)wo, (u32)((size_t)wo >> 32) & 0xffffu, 32768u, 0x00020000u};
#pragma unroll
      for (int ks = 0; ks < 4; ks++) DMA0(lane * 16, (m * 4 + ks) * 1024, lb + 32768 + (m * 4 + ks) * 1024);
    }
  }
  asm volatile("" ::: "memory");
  __builtin_amdgcn_s_waitcnt(0x0070);
  __builtin_amdgcn_s_barrier();
  asm volatile("" ::: "memory");
  STAMP(10);
  if (m < 8) {
    if (which == 1) {
#pragma unroll
      for (int ks = 0; ks < 4; ks++) {
        const u32 a = lb + 32768 + (32 * ks + 8 * g + (i >> 2)) * 256 + m * 32 + (i & 3) * 8;
        const v2u lo = __builtin_bit_cast(v2u, rd(a)), hi = __builtin_bit_cast(v2u, rd(a + 1024));
        B[ks] = v4u{lo[0], lo[1], hi[0], hi[1]};
      }
    }
    float cs = 0.f;
#pragma unroll
    for (int dt = 0; dt < 8; dt++) {
      v4f a = {0, 0, 0, 0};
#pragma unroll
      for (int ks = 0; ks < 4; ks++) a = MFMA(*(const LAS v4u*)(size_t)(lb + (dt * 4 + ks) * 1024 + lane * 16), B[ks], a);
      D[dt] = a; cs += (a[0] + a[1]) + (a[2] + a[3]);
    }
    STAMP(11);
    float c2 = cs; sw16(cs, c2); cs += c2; c2 = cs; sw32(cs, c2); cs = (cs + c2) * (1.f / 128.f);
#pragma unroll
    for (int dt = 0; dt < 8; dt++) D[dt] -= cs;
    float wc = 0.f;
    if (which == 1) {
      float z = wra + wrb; z += DPPF(z, 0x121); z += DPPF(z, 0x122); z += DPPF(z, 0x124); z += DPPF(z, 0x128);
      c2 = z; sw16(z, c2); z += c2; c2 = z; sw32(z, c2); z = (z + c2) * (1.f / 128.f);
      wc = (wrm - z) * LOG2E;
      if (g == 0) L[18432 + 16 * m + i] = wc;
    }
#pragma unroll
    for (int pass = 0; pass < 2; pass++) {
      if (pass == 0 || which == 1) {
        v4f r[8];
#pragma unroll
        for (int dt = 0; dt < 8; dt++) r[dt] = pass ? D[dt] * wc : D[dt];
        __builtin_amdgcn_sched_barrier(0);
#pragma unroll
        for (int st = 0; st < 4; st++) {
#pragma unroll
          for (int dt = 0; dt < 8; dt++)
#pragma unroll
            for (int q = 0; q < 4; q++) { const float z = r[dt][q]; r[dt][q] = z + (st == 0 ? DPPF(z, 0x121) : st == 1 ? DPPF(z, 0x122) : st == 2 ? DPPF(z, 0x124) : DPPF(z, 0x128)); }
          __builtin_amdgcn_sched_barrier(0);
        }
        if (i == 0) {
#pragma unroll
          for (int dt = 0; dt < 8; dt++) *(v4f*)(L + 16384 + pass * 1024 + m * 128 + dt * 16 + 4 * g) = r[dt];
        }
      }
    }
  }
  STAMP(12);
  __syncthreads();
  if (t < 256) {
    float z = 0.f;
#pragma unroll
    for (int q = 0; q < 8; q++) z += L[16384 + (t >> 7) * 1024 + q * 128 + (t & 127)];
    L[t] = z;
  }
  __syncthreads();
  if (which == 0) {
    if (m < 8) {
#pragma unroll
      for (int dt = 0; dt < 8; dt++) D[dt] = (D[dt] - *(const v4f*)(L + dt * 16 + 4 * g) * (1.f / 128.f)) * (LOG2E * 0.08838834764831845f);
    }
  } else if (m < 4) {
    const float* p = L + ((i & 3) == 1 ? 18432 : ((i & 1) ? 128 : 0)) + 32 * m + 4 * g;
    v4f a = *(const v4f*)p, c = *(const v4f*)(p + 16);
    v4u o = {pk2(a[0], a[1]), pk2(a[2], a[3]), pk2(c[0], c[1]), pk2(c[2], c[3])};
    if ((i & 7) == 5) o = v4u{pk2(a[0] - bflo(o[0]), a[1] - bfhi(o[0])), pk2(a[2] - bflo(o[1]), a[3] - bfhi(o[1])), pk2(c[0] - bflo(o[2]), c[1] - bfhi(o[2])), pk2(c[2] - bflo(o[3]), c[3] - bfhi(o[3]))};
    if ((i & 3) == 0) o = v4u{0x3f803f80u, 0x3f803f80u, 0x3f803f80u, 0x3f803f80u};
    *(v4u*)(ws + WS_W + 65536 + m * 1024 + lane * 16) = o;
  }
  STAMP(13);
  if (m < 8) {
#pragma unroll
    for (int s = 0; s < 4; s++) {
      const v4u o = {pk2(D[2 * s][0], D[2 * s][1]), pk2(D[2 * s][2], D[2 * s][3]), pk2(D[2 * s + 1][0], D[2 * s + 1][1]), pk2(D[2 * s + 1][2], D[2 * s + 1][3])};
      *(v4u*)(ws + WS_W + which * 32768 + (m * 4 + s) * 1024 + lane * 16) = o;
    }
  }
}

__global__ void __launch_bounds__(640) k1(const int* __restrict__ src, const int* __restrict__ dst, const u16* __restrict__ x,
                                          const u16* __restrict__ dis, const u16* __restrict__ wq, const u16* __restrict__ wk,
                                          const u16* __restrict__ wv, const u16* __restrict__ wo, char* __restrict__ ws) {
#define KID 0
  __shared__ __attribute__((aligned(16))) u32 L[8704];
  const int t = threadIdx.x, b = blockIdx.x;
  const __amdgpu_buffer_rsrc_t rs = __builtin_amdgcn_make_buffer_rsrc(ws, 0, WS_DBG, 0x00020000);
  STAMP_INIT STAMP(0);
  if (b < 128) {
    u32* stage = L; u32* cnt = L + 8000; u32* novf = L + 8256;
    v4u s4 = {0, 0, 0, 0}, d4 = {0, 0, 0, 0};
    if (t < 625) { s4 = *(const v4u*)(src + b * 2500 + t * 4); d4 = *(const v4u*)(dst + b * 2500 + t * 4); }
    u32 pf = 0;
    if (t >= 632) pf = *(const u32*)(((b & 64) ? ((b & 32) ? wo : wv) : ((b & 32) ? wk : wq)) + (b & 31) * 512 + (t - 632) * 64);
    if (t < 257) cnt[t] = 0;
    if (b == 0 && t == 0) *(u32*)(ws + WS_CNT) = 0;
    __syncthreads();
    STAMP(1);
    u32* ovf = (u32*)(ws + WS_OVF1) + b * 2504;
    if (t < 625) {
      u32 r[4], bin[4], en[4];
#pragma unroll
      for (int i = 0; i < 4; i++) {
        u32 d = d4[i];
        bin[i] = (d * 52429u) >> 21;
        r[i] = atomicAdd(&cnt[bin[i]], 1u);
        en[i] = tabidx(s4[i]) | ((d - bin[i] * 40u) << 16);
      }
#pragma unroll
      for (int i = 0; i < 4; i++) {
        if (r[i] < 31) stage[bin[i] * 32 + 1 + r[i]] = en[i];
        else { u32 o = atomicAdd(novf, 1u); ovf[4 + o] = (en[i] & 0xffffu) | (d4[i] << 16); }
      }
    }
    __syncthreads();
    STAMP(2);
    if (t == 0) ovf[0] = *novf;
    asm volatile("" :: "v"(pf));
#pragma unroll
    for (int it = 0; it < 4; it++) {
      int i = t + it * 640;
      if (i < 2000) {
        int bin = i >> 3, q = i & 7;
        u32 c = cnt[bin];
        if ((u32)(q * 4) <= c) {
          v4u v = *(const v4u*)(stage + bin * 32 + q * 4);
          if (q == 0) v[0] = c;
          __builtin_amdgcn_raw_buffer_store_b128(v, rs, WS_SUB + b * 128 + bin * 16384 + q * 16, 0, AUX1);
        }
      }
    }
    STAMP(3);
  } else {
    const int wb = b - 128;
    char* S = (char*)L;
    v4u v[2]; float dv[2]; u32 go[2];
#pragma unroll
    for (int it = 0; it < 2; it++) {
      int i = t + it * 640; int node = i >> 4, cq = i & 15;
      v[it] = *(const v4u*)(x + (size_t)(wb * 80 + node) * 128 + cq * 8);
      dv[it] = bf2f(dis[wb * 80 + node]);
    }
#pragma unroll
    for (int it = 0; it < 2; it++) {
      int i = t + it * 640; int g = i / 40, pc = i - g * 40;
      go[it] = g * GSTRIDE + (pc < 32 ? (pc >> 4) * 32768 + wb * 256 + (pc & 15) * 16 : 65536 + wb * 128 + (pc - 32) * 16);
    }
#pragma unroll
    for (int it = 0; it < 2; it++) {
      int i = t + it * 640; int node = i >> 4, cq = i & 15;
      int s = node >= 40, l = node - s * 40;
      int pos = l < 32 ? (l >> 4) * 32 + s * 16 + (l & 15) : 32 + s * 8 + l;
      v2u a, c;
      a[0] = pk2s(bflo(v[it][0]) * dv[it], bfhi(v[it][0]) * dv[it]);
      a[1] = pk2s(bflo(v[it][1]) * dv[it], bfhi(v[it][1]) * dv[it]);
      c[0] = pk2s(bflo(v[it][2]) * dv[it], bfhi(v[it][2]) * dv[it]);
      c[1] = pk2s(bflo(v[it][3]) * dv[it], bfhi(v[it][3]) * dv[it]);
      *(v2u*)(S + (2 * cq) * 656 + pos * 8) = a;
      *(v2u*)(S + (2 * cq + 1) * 656 + pos * 8) = c;
    }
    __syncthreads();
    STAMP(2);
#pragma unroll
    for (int it = 0; it < 2; it++) {
      int i = t + it * 640; int g = i / 40, pc = i - g * 40;
      v4u o = *(const v4u*)(S + g * 656 + pc * 16);
      __builtin_amdgcn_raw_buffer_store_b128(o, rs, WS_G + go[it], 0, AUX1);
    }
    const v4u pz = {POISON, POISON, POISON, POISON};
#pragma unroll
    for (int it = 0; it < 4; it++) __builtin_amdgcn_raw_buffer_store_b128(pz, rs, WS_G + go[it & 1], (1 + (it >> 1)) * GBUF, 16);
    STAMP(3);
  }
  STAMP_END;
#undef KID
}

#define B_ELL 0
#define B_CB 1280
#define B_CN 1472
#define B_BUSY 2240
#define B_FULL 2432
#define B_ROWM 2480
#define B_NT 2576
#define B_NF 2577
#define B_Z0 1280
#define B_ZN 1300
#define B_Q 2592
#define B_F 6752
#define B_END 10848
__global__ void __launch_bounds__(1024) k1b(const u16* __restrict__ wq, const u16* __restrict__ wk, const u16* __restrict__ wv, const u16* __restrict__ wo,
                                            const u16* __restrict__ wr, char* __restrict__ ws) {
#define KID 1
  __shared__ __attribute__((aligned(16))) u32 L[18560];
  const int t = threadIdx.x, slot = blockIdx.x, lane = t & 63, w = __builtin_amdgcn_readfirstlane(t >> 6);
  STAMP_INIT STAMP(0);
  if (slot >= 250) {
    if (w >= 12) {
      const __amdgpu_buffer_rsrc_t rp = __builtin_amdgcn_make_buffer_rsrc(ws + WS_G + 3 * GBUF + slot * 40960, 0, 40960, 0x00020000);
      const v4u pz = {POISON, POISON, POISON, POISON};
#pragma unroll
      for (int i = 0; i < 10; i++) __builtin_amdgcn_raw_buffer_store_b128(pz, rp, (t - 768) * 16 + i * 4096, 0, 16);
    } else if (slot < 252) { wprep(slot - 250, wq, wk, wv, wo, wr, ws, (float*)L WSA); STAMP(5); STAMP_END; }
    return;
  }
  const v4u v = *(const v4u*)(ws + WS_SUB + min(slot, 249) * 16384 + t * 16);
  u32 n1 = 0;
  if (slot < 128) n1 = ((const u32*)(ws + WS_OVF1))[slot * 2504];
  if (t < 320) { const v4u z = {POISON, POISON, POISON, POISON}; *(v4u*)(L + B_ELL + t * 4) = z; }
  else if (t < 320 + B_ZN / 4) { const v4u z = {0, 0, 0, 0}; *(v4u*)(L + B_Z0 + (t - 320) * 4) = z; }
  LBAR();
  STAMP(1);
  u32* const gcnt = (u32*)(ws + WS_CNT);
  u32* const ovf2 = (u32*)(ws + WS_OVF2);
  {
    const u32 q = t & 7;
    u32 c = DPPU(v[0], 0x00);
    c = (u32)__builtin_amdgcn_update_dpp((int)c, (int)c, 0x114, 0xF, 0xA, false);
    c = min(c, 31u);
    u32 inc = c + (u32)__builtin_amdgcn_update_dpp(0, (int)c, 0x118, 0xF, 0xC, false);
    inc += (u32)__builtin_amdgcn_update_dpp(0, (int)inc, 0x142, 0xA, 0xF, false);
    inc += (u32)__builtin_amdgcn_update_dpp(0, (int)inc, 0x143, 0xC, 0xF, false);
    u32 base = 0;
    if (lane == 63) base = atomicAdd(&L[B_NT], inc);
    base = __builtin_amdgcn_readlane(base, 63);
    const u32 pos = B_Q + base + inc - c + 4 * q - 1;
#pragma unroll
    for (int i = 0; i < 4; i++) {
      const u32 id = 4 * q + i;
      L[(id - 1u < c) ? pos + i : B_Q + 4096 + lane] = v[i];
    }
  }
  if (n1) {
    const u32* o1 = (const u32*)(ws + WS_OVF1) + slot * 2504;
    for (u32 i = t; i < n1; i += 1024) ovf2[atomicAdd(gcnt, 1u)] = o1[4 + i];
  }
  LBAR();
  STAMP(2);
  if (w >= 12) {
    const __amdgpu_buffer_rsrc_t rp = __builtin_amdgcn_make_buffer_rsrc(ws + WS_G + 3 * GBUF + slot * 40960, 0, 40960, 0x00020000);
    const v4u pz = {POISON, POISON, POISON, POISON};
    if (w >= 12) {
#pragma unroll
      for (int i = 0; i < 10; i++) __builtin_amdgcn_raw_buffer_store_b128(pz, rp, (t - 768) * 16 + i * 4096, 0, 16);
    }
    return;
  }
  auto place = [&](u32 e, u32 r, u32 c) {
    const u32 loc = e >> 16, hp = loc >> 3;
    const u32 j = hp == 4 ? 24 + (r >> 1) : (r < 12 ? (hp >> 1) * 12 + r : 20 + (hp >> 1) * 4 + r);
    const u32 half = (hp == 4 ? r : hp) & 1;
    const u32 ln = half * 32 + (loc & 4) * 4 + c * 4 + (loc & 3);
    ((u16*)(L + B_ELL))[(j >> 3) * 512 + ln * 8 + (j & 7)] = (u16)e;
    atomicOr(&L[B_ROWM + half * 40 + j], 1u << ((e + 16) & 31));
  };
  const u32 tot = L[B_NT];
  for (u32 b0 = w * 128; b0 < tot; b0 += 1536) {
    u32 e[2], hb[2], rk[2], r[2], c[2];
#pragma unroll
    for (int i = 0; i < 2; i++) { const u32 id = b0 + i * 64 + lane; e[i] = L[B_Q + id]; if (id >= tot) e[i] = ((40 + (lane & 7)) << 16) | (lane & 31); }
#pragma unroll
    for (int i = 0; i < 2; i++) { hb[i] = (e[i] >> 19) * 32 + (e[i] & 31); rk[i] = atomicAdd(&L[B_CB + hb[i]], 1u); }
#pragma unroll
    for (int i = 0; i < 2; i++) {
      r[i] = e[i] >= (32u << 16) ? (rk[i] + (e[i] >> 1)) & 15 : (rk[i] < 12 ? rk[i] : rk[i] - 12);
      c[i] = atomicAdd(&L[rk[i] < 24 ? B_CN + (e[i] >> 16) * 16 + r[i] : B_CN + 640 + lane], 1u);
    }
#pragma unroll
    for (int i = 0; i < 2; i++) {
      if (e[i] < (40u << 16)) {
        if (rk[i] < 24 && c[i] < 4) {
          place(e[i], r[i], c[i]);
          atomicOr(&L[B_BUSY + hb[i]], 1u << r[i]);
          if (c[i] == 3) atomicOr(&L[B_FULL + (e[i] >> 16)], 1u << r[i]);
        } else L[B_F + atomicAdd(&L[B_NF], 1u)] = e[i];
      }
    }
  }
  LBAR();
  STAMP(3);
  const u32 nf = L[B_NF];
  for (u32 id = t; id < nf; id += 768) {
    const u32 e = L[B_F + id], loc = e >> 16, hb = (e >> 19) * 32 + (e & 31);
    u32 rho = (id * 5 + (id >> 2)) & 15, tried = 0;
    const u32 pm = loc >= 32 ? 0xFFFFu : 0xFFFu;
    for (int round = 0;; round++) {
      const u32 bs = __hip_atomic_load(&L[B_BUSY + hb], __ATOMIC_RELAXED, __HIP_MEMORY_SCOPE_WORKGROUP);
      const u32 nfl = ~(__hip_atomic_load(&L[B_FULL + loc], __ATOMIC_RELAXED, __HIP_MEMORY_SCOPE_WORKGROUP) | tried);
      u32 cand = nfl & ~bs & pm;
      const bool strict = cand != 0 && round < 2;
      if (!cand) cand = nfl & pm;
      if (!cand) cand = nfl & ~bs & 0xF000;
      if (!cand) cand = nfl & 0xF000;
      if (!cand) { ovf2[atomicAdd(gcnt, 1u)] = (e & 0xffffu) | ((slot * 40 + loc) << 16); break; }
      const u32 r = (rho + __builtin_ctz((cand | (cand << 16)) >> rho)) & 15;
      const u32 ob = atomicOr(&L[B_BUSY + hb], 1u << r);
      const u32 c = atomicAdd(&L[B_CN + loc * 16 + r], 1u);
      if (c == 3) atomicOr(&L[B_FULL + loc], 1u << r);
      if (c < 4 && !(strict && ((ob >> r) & 1))) { place(e, r, c); break; }
      if (c >= 3) tried |= 1u << r;
      rho = (rho + 7) & 15;
    }
  }
  LBAR();
  STAMP(4);
  if (t < 320) {
    v4u o = *(const v4u*)(L + B_ELL + t * 4);
    const u32* rm = L + B_ROWM + (lane >> 5) * 40 + w * 8;
    const v4u m0 = *(const v4u*)rm, m1 = *(const v4u*)(rm + 4);
#pragma unroll
    for (int i = 0; i < 4; i++) {
      const u32 a = i < 2 ? m0[2 * i] : m1[2 * i - 4], bb = i < 2 ? m0[2 * i + 1] : m1[2 * i - 3];
      u32 fa, fb;
      asm("v_ffbl_b32 %0, %1" : "=v"(fa) : "v"(~a)); asm("v_ffbl_b32 %0, %1" : "=v"(fb) : "v"(~bb));
      v2h d = {(u16)(fa + ZROW), (u16)(fb + ZROW)};
      const u32 oi = o[i];
      o[i] = __builtin_bit_cast(u32, __builtin_elementwise_min(__builtin_bit_cast(v2h, oi), d));
    }
    const __amdgpu_buffer_rsrc_t rs = __builtin_amdgcn_make_buffer_rsrc(ws, 0, WS_DBG, 0x00020000);
    __builtin_amdgcn_raw_buffer_store_b128(o, rs, WS_ELL + slot * 5120 + t * 16, 0, AUX1);
  }
  STAMP(5);
  STAMP_END;
#undef KID
}

__device__ __forceinline__ v16bf rd4(u32 a0, u32 a1, u32 a2, u32 a3) {
  const v4bf a = rd(a0), b = rd(a1), c = rd(a2), d = rd(a3);
  return __builtin_shufflevector(__builtin_shufflevector(a, b, 0, 1, 2, 3, 4, 5, 6, 7), __builtin_shufflevector(c, d, 0, 1, 2, 3, 4, 5, 6, 7),
                                 0, 1, 2, 3, 4, 5, 6, 7, 8, 9, 10, 11, 12, 13, 14, 15);
}
#define DMA(voff, soff, lbase) asm volatile("s_mov_b32 m0, %2\n\ts_nop 0\n\tbuffer_load_dwordx4 %0, %3, %1 offen sc1 lds" :: "v"(voff), "s"(soff), "s"(lbase), "s"(srd) : "memory")
#define PFM 1
#define PR1 0
#define PR2 2
#define PR3 4
#define NPRE 1
#define KMAX 7
#define LASTP 6
#define D23POS 0
#define D4POS 2
#define T2PRE
__global__ void __launch_bounds__(1024) k2(const u16* __restrict__ dis, char* __restrict__ ws) {
#define KID 2
  __shared__ __attribute__((aligned(16))) char T[163840];
  const int t = threadIdx.x, lane = t & 63, w = __builtin_amdgcn_readfirstlane(t >> 6);
  const int b = blockIdx.x, gp = (b & 7) * 2 + (b >> 7), p = (b >> 3) & 15;
  const int slot = p * 16 + w;
  const bool act = slot < 250;
  asm volatile("" :: "v"((u32)(size_t)(LAS char*)T));
  STAMP_INIT STAMP(0);
  const __amdgpu_buffer_rsrc_t rs = __builtin_amdgcn_make_buffer_rsrc(ws + WS_G, 0, 8u * GBUF, 0x00020000);
  const v4u srd = {(u32)(size_t)(ws + WS_G), (u32)((size_t)(ws + WS_G) >> 32) & 0xffffu, 8u * GBUF, 0x00020000u};
  const u32 sgA = gp * 2 * GSTRIDE;
  const u32 vla = w * 1024 + lane * 16;
  const u32 vlc = (w == 15 && lane >= 40) ? OOB : vla;
  const u32 lb0 = w * 1024, lb2 = w * 1024 + 32768 + (w >= 8 ? 40960 : 0);
  DMA(vla, sgA, lb0); DMA(vla, sgA + 16384, lb0 + 16384); DMA(vla, sgA + 32768, lb2); DMA(vla, sgA + 49152, lb0 + 90112); DMA(vlc, sgA + 65536, lb0 + 106496);
  v4u e[5]; u16 dvr[3];
  const int sl = act ? slot : 0;
#pragma unroll
  for (int i = 0; i < 5; i++) e[i] = *(const v4u*)(ws + WS_ELL + sl * 5120 + i * 1024 + lane * 16);
  dvr[0] = dis[sl * 40 + (lane >> 2)]; dvr[1] = dis[sl * 40 + 16 + (lane >> 2)]; dvr[2] = dis[sl * 40 + 32 + ((lane & 31) >> 2)];
  const u32 novf = *(const u32*)(ws + WS_CNT);
  const u32 vp01 = act ? slot * 128 + (lane >> 1) * 4 : OOB;
  const u32 vp2 = act ? 65536 + slot * 64 + (lane >> 1) * 4 : OOB;
  v8bf sel1, sel2; const int seli = (lane & 1) ? 0xEEEEEEEE : 0x44444444;
  {
    const u32 on = (lane & 2) ? 0u : 0x3f803f80u, lo = (lane & 4) ? 0u : on, hi = (lane & 4) ? on : 0u;
    const bool m1 = ((lane >> 4) & 1) == ((lane >> 3) & 1);
    const v4u a2 = {lo, lo, hi, hi}, a1 = {m1 ? lo : 0u, m1 ? lo : 0u, m1 ? hi : 0u, m1 ? hi : 0u};
    sel1 = __builtin_bit_cast(v8bf, a1); sel2 = __builtin_bit_cast(v8bf, a2);
  }
  u32 vchk;
  {
    const u32 i = (lane >> 3) & 3, ln = (w == 15 && (i & 1)) ? (lane & 1) : (lane & 7);
    const u32 hl = (w == 15 && (lane & 15) >= 10) ? (lane & 7) : (lane & 15);
    vchk = lane < 32 ? (i == 2 ? lb2 : lb0 + (i == 1 ? 16384 : (i == 3 ? 90112 : 0))) + ln * 128 + lane * 4 : lb0 + 106496 + hl * 64 + (lane & 15) * 4;
  }
  if (t < 32) { const v4u z = {0, 0, 0, 0}; *(LAS v4u*)(size_t)(ZROW * 8 + (t >> 4) * 40960 + (t & 15) * 16) = z; }
  u32 ad[32];
#pragma unroll
  for (int i = 0; i < 16; i++) { const u32 q = e[i >> 2][i & 3]; ad[2 * i] = (q & 0xffffu) * 8; ad[2 * i + 1] = (q >> 16) * 8; }
  u32 ex[4] = {e[4][0], e[4][1], e[4][2], e[4][3]};
  bool xm;
  {
    const u32 m0 = min(min(ex[0] & 0xffffu, ex[0] >> 16), min(ex[1] & 0xffffu, ex[1] >> 16));
    const u32 m1 = min(min(ex[2] & 0xffffu, ex[2] >> 16), min(ex[3] & 0xffffu, ex[3] >> 16));
    xm = __builtin_amdgcn_ballot_w64(min(m0, m1) < ZROW) != 0;
  }
  float dv2[3];
#pragma unroll
  for (int i = 0; i < 3; i++) { const float d = bf2f(dvr[i]); dv2[i] = d * d; }
  asm volatile("" ::: "memory");
  __builtin_amdgcn_s_waitcnt(0x0070);
  __builtin_amdgcn_s_barrier();
  asm volatile("" ::: "memory");
  STAMP(1);

  auto slow = [&](const u32 tofs, const u32 n0, const u32 nn) __attribute__((always_inline)) -> float {
    const u32* ov = (const u32*)(ws + WS_OVF2);
    float a = 0.f;
    for (u32 base = 0; base < novf; base += 64) {
      const u32 en = base + lane < novf ? ov[base + lane] : POISON;
      u64 m = __builtin_amdgcn_ballot_w64((en >> 16) - n0 < nn);
      while (m) {
        const int l = __builtin_ctzll(m); m &= m - 1;
        const u32 es = __builtin_amdgcn_readlane(en, l);
        const float val = bf2f(*(LAS u16*)(size_t)((es & 0xffffu) * 8 + tofs + (lane & 3) * 2));
        a += ((es >> 16) - n0 == (u32)(lane >> 2)) ? val : 0.f;
      }
    }
    return a;
  };
  auto grow = [&](const u32 la, const u32 scur) __attribute__((always_inline)) -> v2u {
    v2u v = {0, 0};
    if (la < ZROW * 8) {
      u32 spins = 0;
      do {
        v = __builtin_amdgcn_raw_buffer_load_b64(rs, la >= 81920 ? la - 40960 : la, scur, 16);
        asm volatile("" ::: "memory");
      } while (max(v[0], v[1]) == POISON && ++spins < MAXSPIN);
    }
    return v;
  };
  auto fallback = [&](const int bt, const u32 scur) __attribute__((always_inline)) -> float {
    float s0 = 0.f, s1 = 0.f, s2 = 0.f, s3 = 0.f;
#pragma unroll 1
    for (int j = 0; j < (bt < 2 ? 16 : 8); j++) {
      const int je = j < 12 ? bt * 12 + j : 20 + bt * 4 + j;
      const u32 la = *(const u16*)(ws + WS_ELL + sl * 5120 + (je >> 3) * 1024 + lane * 16 + (je & 7) * 2) * 8u;
      const v2u v = grow(la, scur);
      s0 += bflo(v[0]); s1 += bfhi(v[0]); s2 += bflo(v[1]); s3 += bfhi(v[1]);
    }
    float a = 0.f;
#pragma unroll
    for (int c = 0; c < 4; c++) {
      const int sl_ = (lane & 48) + 4 * c + ((lane >> 2) & 3);
      const float t0 = __shfl(s0, sl_), t1 = __shfl(s1, sl_), t2 = __shfl(s2, sl_), t3 = __shfl(s3, sl_);
      a += (lane & 2) ? ((lane & 1) ? t3 : t2) : ((lane & 1) ? t1 : t0);
    }
    if (bt == 2) a += __shfl_xor(a, 32);
    const u32* ov = (const u32*)(ws + WS_OVF2);
    const u32 n0 = slot * 40 + bt * 16;
    for (u32 i = 0; i < novf; i++) {
      const u32 es = ov[i];
      if ((es >> 16) - n0 == (u32)(lane >> 2) && (bt < 2 || lane < 32)) {
        const v2u v = grow((es & 0xffffu) * 8, scur);
        const u32 q = (lane & 2) ? v[1] : v[0];
        a += (lane & 1) ? bfhi(q) : bflo(q);
      }
    }
    return a;
  };
  auto tail = [&](const int bt, const v4f acc, const u32 scur, const u32 voff, const u32 soff) __attribute__((always_inline)) {
    float a = acc[0] + acc[1];
    if (__builtin_expect(__builtin_amdgcn_ballot_w64(a != a) != 0, 0)) a = fallback(bt, scur);
    const float d2 = dv2[bt];
    const float tn = __builtin_bit_cast(float, DPPU(__float_as_uint(a), 0xB1)) * d2;
    const u32 cm = min(pk2(a * d2, tn), 0xFFFFFFFEu);
    asm volatile("s_mov_b64 exec, %4\n\tbuffer_store_dword %0, %1, %2, %3 offen sc1\n\ts_mov_b64 exec, -1"
                 :: "v"(cm), "v"(voff), "s"(srd), "s"(soff), "s"(bt < 2 ? 0x5555555555555555ull : 0x55555555ull) : "memory");
  };
  v4f acc = {0, 0, 0, 0}, acc2 = {0, 0, 0, 0};
  u32 vp2x = OOB;
  auto phase = [&](auto GBc, auto XMc, const bool recv, const u32 sprev, const u32 scur, const u32 srecv_, const int sb) __attribute__((always_inline)) {
    const u32 srecv = srecv_;
    constexpr u32 GB = decltype(GBc)::value;
    constexpr bool XM = decltype(XMc)::value;
    constexpr u32 to = GB * 40960u, ro = (1 - GB) * 40960u;
    constexpr int NQ = XM ? 10 : 8, E0 = XM ? 3 : 2, E1 = XM ? 7 : 5;
    __builtin_amdgcn_s_setprio(3);
    if (recv) { DMA(vla, srecv, lb0 + ro); DMA(vla, srecv + 16384, lb0 + 16384 + ro); }
    auto grp = [&](const int s) __attribute__((always_inline)) -> v16bf {
      if (XM && (s == E0 || s == E1)) {
        u32 q0 = ex[s == E0 ? 0 : 2], q1 = ex[s == E0 ? 1 : 3];
        asm volatile("" : "+v"(q0), "+v"(q1));
        return rd4((q0 & 0xffffu) * 8 + to, (q0 >> 16) * 8 + to, (q1 & 0xffffu) * 8 + to, (q1 >> 16) * 8 + to);
      }
      const int q = XM ? s - (s > E0) - (s > E1) : s;
      return rd4(ad[4 * q] + to, ad[4 * q + 1] + to, ad[4 * q + 2] + to, ad[4 * q + 3] + to);
    };
    v16bf g[3];
#pragma unroll
    for (int q = 0; q < 3; q++) g[q] = grp(q);
    __builtin_amdgcn_sched_barrier(0);
    if (D23POS < 0 && recv) { DMA(vla, srecv + 32768, lb2 + ro); DMA(vla, srecv + 49152, lb0 + 90112 + ro); }
    float e0 = 0.f, e1 = 0.f, e2 = 0.f;
    if (__builtin_expect(novf != 0, 0)) { e0 = slow(to, slot * 40, 16); e1 = slow(to, slot * 40 + 16, 16); e2 = slow(to, slot * 40 + 32, 8); }
    __builtin_amdgcn_sched_barrier(0);
    u32 chk = 0;
#pragma unroll
    for (int q = 0; q < NQ; q++) {
      if (q == 0) { acc[0] = e0; acc[1] = 0.f; }
      if (q == E0 + 1) { acc[0] = e1; acc[1] = 0.f; }
      if (q == E1 + 1) { acc2[0] = e2; acc2[1] = 0.f; }
      if (q == NQ - 1 && recv) {
        asm volatile("s_waitcnt vmcnt(%0)" :: "n"(D4POS <= E0 ? 2 : 1) : "memory");
        chk = *(volatile LAS u32*)(size_t)(vchk + ro);
      }
      if (q <= E1) acc = __builtin_amdgcn_smfmac_f32_16x16x64_bf16(sel1, g[q % 3], acc, seli, 0, 0);
      else acc2 = __builtin_amdgcn_smfmac_f32_16x16x64_bf16(sel2, g[q % 3], acc2, seli, 0, 0);
      if (q + 3 < NQ) g[q % 3] = grp(q + 3);
      if (q == D23POS && recv) { DMA(vla, srecv + 32768, lb2 + ro); DMA(vla, srecv + 49152, lb0 + 90112 + ro); }
      if (q == D4POS && recv) DMA(vlc, srecv + 65536, lb0 + 106496 + ro);
      if (q == PR1) __builtin_amdgcn_s_setprio(2);
      if (q == PR2) __builtin_amdgcn_s_setprio(1);
      if (q == PR3) __builtin_amdgcn_s_setprio(0);
      __builtin_amdgcn_sched_barrier(0);
      if (q == E0 || q == E1) {
        tail(q == E1, acc, scur, vp01, scur + GBUF + (q == E1 ? 32768 : 0));
        __builtin_amdgcn_sched_barrier(0);
      }
    }
    tail(2, acc2, scur, vp2, scur + GBUF);
    PSTAMP(sb);
    if (recv) {
      u32 spins = 0;
      if (__builtin_expect(__builtin_amdgcn_ballot_w64(chk == POISON) != 0, 0)) {
        do {
          spins++;
          DMA(vla, srecv, lb0 + ro); DMA(vla, srecv + 16384, lb0 + 16384 + ro);
          DMA(vla, srecv + 32768, lb2 + ro); DMA(vla, srecv + 49152, lb0 + 90112 + ro);
          DMA(vlc, srecv + 65536, lb0 + 106496 + ro);
          asm volatile("s_waitcnt vmcnt(0)" ::: "memory");
        } while (__builtin_amdgcn_ballot_w64(*(volatile LAS u32*)(size_t)(vchk + ro) == POISON) != 0 && spins < MAXSPIN);
      }
      PSTAMP(sb + 1);
      asm volatile("" ::: "memory");
      __builtin_amdgcn_s_barrier();
      asm volatile("" ::: "memory");
      PSTAMP(sb + 2);
    }
  };
  using F0 = std::integral_constant<u32, 0>; using F1 = std::integral_constant<u32, 1>;
  auto hops = [&](auto XMc) __attribute__((always_inline)) {
    u32 sk = sgA;
#pragma unroll 1
    for (int k = 1; k <= KMAX; k++) {
      phase(F0{}, XMc, true, sk - GBUF + GSTRIDE, sk, sk + GSTRIDE, 6 * k - 4);
      phase(F1{}, XMc, k < KMAX, sk, sk + GSTRIDE, sk + GBUF, 6 * k - 1);
      sk += GBUF;
    }
  };
  if (xm) hops(std::true_type{}); else hops(std::false_type{});
  STAMP(47);
  STAMP_END;
#undef KID
}

#define L_WR 32768u
#define L_WW 81920u
__device__ __forceinline__ u32 chunk(u32 id) { return 40960u + id * 2048u + (id >= 20u ? 40960u : 0u); }
__device__ __forceinline__ v4u ld128(u32 a) { return *(const LAS v4u*)(size_t)a; }
__device__ __forceinline__ v2u ld64(u32 a) { return *(const LAS v2u*)(size_t)a; }
__device__ __forceinline__ float bperm(u32 a, float v) { return __uint_as_float((u32)__builtin_amdgcn_ds_bpermute((int)a, (int)__float_as_uint(v))); }
__device__ __forceinline__ float dot2(u32 a, u32 b, float c) { return __builtin_amdgcn_fdot2_f32_bf16(__builtin_bit_cast(v2bf, a), __builtin_bit_cast(v2bf, b), c, false); }
#define RD 3
#define SB() __builtin_amdgcn_sched_barrier(0)
template <int I, int E, class F> __device__ __forceinline__ void sfor(F&& fn) {
  if constexpr (I < E) { fn(std::integral_constant<int, I>{}); sfor<I + 1, E>(fn); }
}
#define fmx(a, b) __builtin_amdgcn_fmed3f((a), (b), finf)
#define PS1 24
#define PS2 48
#define FO(mm) ((mm) == 0 ? 39 : 38 + 4 * (mm) - ((mm) == 7))
#define OPQ(x) asm volatile("" : "+v"(x))
#define OPQ2(x, y) asm volatile("" : "+v"(x), "+v"(y))
__device__ __forceinline__ float sel(bool c, float a, float b) { asm("" : "+v"(a)); return c ? a : b; }
__device__ __forceinline__ u32 pk2o(float a, float b) { asm("" : "+v"(a)); asm("" : "+v"(b)); return pk2(a, b); }
struct TfPre { u32 aT, aR, oo, bp, bq; };
__device__ __forceinline__ TfPre tfpre(const u32 j0, const u32 slot, const u32 lane) {
  const u32 g = lane >> 4, n = lane & 15, k = n >> 1, nd = n & 1, u = j0 >> 2, kr = 2 * g + ((n >> 2) & 1);
  TfPre p;
  p.aT = chunk(u * 8 + k) + (g ^ ((k >> 2) << 1)) * 64 + ((j0 ^ k) & 3) * 16 + nd * 8;
  p.aR = chunk(u * 8 + kr) + ((n & 3) ^ ((kr >> 2) << 1)) * 64 + ((j0 ^ kr) & 3) * 16 + (n >> 3) * 8;
  p.oo = (slot * 40 + 2 * j0 + nd) * 256 + k * 32 + 8 * g;
  p.bp = (20 * g + nd) * 4; p.bq = (16 * (n >> 2) + n) * 4;
  OPQ2(p.aT, p.aR); OPQ2(p.bp, p.bq); OPQ(p.oo);
  return p;
}
template <int NT, int PQ>
__device__ __forceinline__ void tf(const TfPre& P, const u32 lane, const float dv0, const float dv1, u16* __restrict__ out) {
  const u32 g = lane >> 4, n = lane & 15, k = n >> 1, nd = n & 1;
  const u32 aT = P.aT, bp = P.bp, bq = P.bq, oo = P.oo;
  u32 aW = lane * 16;
  float finf = __builtin_inff(); asm("" : "+s"(finf));
  v4u TF[NT][4], W[RD];
#pragma unroll
  for (int s = 0; s < 4; s++) {
#pragma unroll
    for (int t = 0; t < NT; t++) {
      const v2u a = ld64((aT ^ (16 * t)) + 512 * s), b = ld64((aT ^ (16 * t)) + 512 * s + 256);
      TF[t][s] = v4u{a[0], a[1], b[0], b[1]};
    }
    if (s < RD) W[s] = ld128(aW + L_WR + s * 1024);
  }
  SB();
  u32 aR[2] = {P.aR, P.aR ^ 16};
  OPQ(aR[1]);
  const float dve[2] = {dv0, dv1};
  float rho[NT], rb[NT][2], c1[NT][2], c3[NT][2], v0[NT], v1[NT], mx[2], zs[2], l1[2], l3[2], q2[2][2];
  v4f S[NT], LN[NT], GR[NT], U[NT][8], y[NT][8], o[NT][8];
  v4u ub[NT], B[NT]; v2u Y[NT][8], tr[NT][8]; u32 dg0[NT], dg1[NT];
  const v4f Z4 = {0, 0, 0, 0};
  SB();
  sfor<-4, 68>([&](auto Fc) __attribute__((always_inline)) {
    constexpr int f = decltype(Fc)::value, m = (f >> 2) & 7, s = f & 3, fn = f + RD;
    if constexpr (f == -4) __builtin_amdgcn_s_setprio(PQ & 3);
    if constexpr (f == PS1) __builtin_amdgcn_s_setprio((PQ >> 4) & 3);
    if constexpr (f == PS2) __builtin_amdgcn_s_setprio((PQ >> 8) & 3);
    if constexpr (f >= 10 && f <= 34 && (f & 7) == 2) {
#pragma unroll
      for (int t = 0; t < NT; t++) S[t] = MFMA(TF[t][(f - 10) >> 3], ub[t], (f > 10 ? S[t] : Z4));
      SB();
    }
#pragma unroll
    for (int mm = 0; mm < 8; mm++) {
      if (f == FO(mm)) {
#pragma unroll
        for (int t = 0; t < NT; t++) o[t][mm] = MFMA((v4u{tr[t][mm][0], tr[t][mm][1], Y[t][mm][0], Y[t][mm][1]}), B[t], Z4);
        SB();
      }
    }
    if constexpr (f < 64) {
      const v4u wf = W[(f + 4) % RD];
#pragma unroll
      for (int t = 0; t < NT; t++) {
        if constexpr (f < 0) { GR[t] = MFMA(TF[t][s], TF[t][s], (s ? GR[t] : Z4)); LN[t] = MFMA(wf, TF[t][s], (s ? LN[t] : Z4)); }
        else if constexpr (f < 32) U[t][m] = MFMA(wf, TF[t][s], (s ? U[t][m] : Z4));
        else y[t][m] = MFMA(TF[t][s], wf, (s ? y[t][m] : Z4));
      }
      if constexpr (fn == 32) { aW += L_WW; OPQ(aW); }
      if constexpr (fn < 64)
      W[(f + 4) % RD] = ld128(aW + (fn < 0 ? L_WR + (fn + 4) * 1024 : (fn & 31) * 1024));
    }
#pragma unroll
    for (int t = 0; t < NT; t++) {
      if (f == 1 + 2 * t) {
        const float qa = sel(nd, GR[t][1], GR[t][0]), qb = sel(nd, GR[t][3], GR[t][2]), q = (n & 2) ? qb : qa;
        const float a = LN[t][0] * (1.f / 128.f);
        rho[t] = fmaxf(q * (1.f / 128.f) - a * a, 0.f) + (k == 0 ? 1e-5f : 1e-5f * dve[t] * dve[t]);
        OPQ(rho[t]);
      }
      if (f == 2 + 2 * t) {
        rho[t] = __builtin_amdgcn_rsqf(rho[t]); rho[t] = k == 0 ? rho[t] : __builtin_copysignf(rho[t], dve[t]);
        const float dd = g < 2 ? (k == 0 ? dve[t] : 1.f) : 0.f;
        l1[t] = 0.5f * dd * LN[t][0]; l3[t] = dd * LN[t][1];
        OPQ2(l1[t], l3[t]);
      }
      if (f == 6 + t) {
        rb[t][0] = bperm(bp, rho[t]); rb[t][1] = bperm(bp + 8, rho[t]); rho[t] = bperm(bq, rho[t]);
      }
      if (f == 8 + t) {
        const u32 d1 = k == 0 ? __float_as_uint(dve[t]) : 0x3f800000u, d2 = (k >> 1) == g ? d1 : 0u, dg = d2 >> ((n & 2) ? 0 : 16);
        dg0[t] = nd ? 0u : dg; dg1[t] = nd ? dg : 0u; OPQ2(dg0[t], dg1[t]);
      }
      if (f == 41) { c1[t][0] = bperm(bp, LN[t][2]); c1[t][1] = bperm(bp + 8, LN[t][2]); c3[t][0] = bperm(bp, LN[t][3]); c3[t][1] = bperm(bp + 8, LN[t][3]); }
      if (f == 12) { v0[t] = rb[t][0] * rho[t]; v1[t] = rb[t][1] * rho[t]; OPQ2(v0[t], v1[t]); rb[t][0] *= dve[t]; rb[t][1] *= dve[t]; OPQ2(rb[t][0], rb[t][1]); }
    }
    if constexpr (f >= 5 && f <= 33 && s == 1) {
      constexpr int mm = (f - 5) >> 2;
#pragma unroll
      for (int t = 0; t < NT; t++) { ub[t][(mm & 1) * 2] = pk2(U[t][mm][0], U[t][mm][1]); ub[t][(mm & 1) * 2 + 1] = pk2(U[t][mm][2], U[t][mm][3]); }
    }
    if (f == 35) {
#pragma unroll
      for (int t = 0; t < NT; t++) { v0[t] *= sel(nd, S[t][1], S[t][0]); v1[t] *= sel(nd, S[t][3], S[t][2]); OPQ2(v0[t], v1[t]); mx[t] = fmx(v0[t], v1[t]); }
      if (NT == 1) mx[1] = mx[0];
      sw16(mx[0], mx[1]); mx[0] = fmx(mx[0], mx[1]);
    }
    if (f == 36) { mx[1] = mx[0]; sw32(mx[0], mx[1]); mx[0] = fmx(mx[0], mx[1]); mx[1] = mx[0]; if (NT == 2) sw16(mx[0], mx[1]); }
    if (f == 36) {
#pragma unroll
      for (int t = 0; t < NT; t++) { v0[t] = __builtin_amdgcn_exp2f(v0[t] - mx[t]); v1[t] = __builtin_amdgcn_exp2f(v1[t] - mx[t]); zs[t] = v0[t] + v1[t]; }
      if (NT == 1) zs[1] = zs[0];
    }
    if (f == 37) { sw16(zs[0], zs[1]); zs[0] += zs[1]; zs[1] = zs[0]; sw32(zs[0], zs[1]); zs[0] = __builtin_amdgcn_rcpf(zs[0] + zs[1]); zs[1] = zs[0]; if (NT == 2) sw16(zs[0], zs[1]); }
    if (f == 38) {
#pragma unroll
      for (int t = 0; t < NT; t++) {
        v0[t] *= rb[t][0]; v1[t] *= rb[t][1]; OPQ(v0[t]); v0[t] *= zs[t]; v1[t] *= zs[t];
        const u32 p = pk2o(v0[t], v1[t]);
        B[t] = v4u{dg0[t], dg1[t], nd ? 0u : p, nd ? p : 0u};
      }
    }
    if (f == 44) {
#pragma unroll
      for (int t = 0; t < NT; t++) {
        l1[t] += v0[t] * c1[t][0]; l3[t] += v0[t] * c3[t][0]; OPQ2(l1[t], l3[t]); l1[t] += v1[t] * c1[t][1]; l3[t] += v1[t] * c3[t][1];
        OPQ2(l1[t], l3[t]);
      }
    }
    if (f == 48) {
#pragma unroll
      for (int t = 0; t < NT; t++) { sw32(l1[t], l3[t]); l1[t] += l3[t]; }
      if (NT == 1) l1[1] = l1[0];
      sw16(l1[0], l1[1]); l1[0] += l1[1];
    }
    if (f == 52) { l3[0] = l1[0]; sw32(l1[0], l3[0]); l1[0] *= 1.f / 128.f; OPQ2(l1[0], l3[0]); }
#pragma unroll
    for (int mm = 0; mm < 8; mm++) {
      const int fo = FO(mm);
      if (f == fo - 3) {
#pragma unroll
        for (int t = 0; t < NT; t++) tr[t][mm] = __builtin_bit_cast(v2u, rd(aR[t] + 256 * mm));
      }
      if (f == 37 + 4 * mm - (mm == 7)) {
#pragma unroll
        for (int t = 0; t < NT; t++) Y[t][mm] = v2u{pk2(y[t][mm][0], y[t][mm][2]), pk2(y[t][mm][1], y[t][mm][3])};
      }
#pragma unroll
      for (int h = 0; h < 2; h++) {
        if (f == fo + 1 + h) {
#pragma unroll
          for (int t = 0; t < NT; t++) {
            const float a = o[t][mm][2 * h], c = o[t][mm][2 * h + 1];
            if (mm == 0 && h == 0) { q2[t][0] = a * a; q2[t][1] = c * c; } else { q2[t][0] += a * a; q2[t][1] += c * c; }
            OPQ2(q2[t][0], q2[t][1]);
          }
        }
      }
    }
    SB();
  });
#pragma unroll
  for (int m = 0; m < 8; m++)
#pragma unroll
    for (int t = 0; t < NT; t++) tr[t][m] = __builtin_bit_cast(v2u, rd(aR[t] + 256 * m));
  const float dr = (NT == 2 && (g & 1)) ? dve[1] : dve[0];
  float al;
  {
    float qa = q2[0][0] + q2[0][1], qb = q2[NT - 1][0] + q2[NT - 1][1];
    sw16(qa, qb); qa += qb; qb = qa; sw32(qa, qb);
    const float var = fmaxf((qa + qb) * (1.f / 128.f) - l1[0] * l1[0], 0.f);
    const float lg = l3[0] * __builtin_copysignf(__builtin_amdgcn_rsqf(var + 1e-5f * dr * dr), dr);
    float mm = fmx(lg, DPPF(lg, 0x122)); mm = fmx(mm, DPPF(mm, 0x124)); mm = fmx(mm, DPPF(mm, 0x128));
    const float e = __builtin_amdgcn_exp2f(lg - mm);
    float z = e + DPPF(e, 0x122); z += DPPF(z, 0x124); z += DPPF(z, 0x128);
    al = e * __builtin_amdgcn_rcpf(z);
  }
  const float ald = al * __builtin_amdgcn_rcpf(dr), als = k == 0 ? al : ald;
  float aa[2] = {ald, ald}; if (NT == 2) sw16(aa[0], aa[1]);
  const u32 br = (4 * g + nd) * 4;
  v4f R[NT][2];
#pragma unroll
  for (int t = 0; t < NT; t++) {
    const float a0 = bperm(br + 64 * t, als), a1 = bperm(br + 64 * t + 8, als);
    const u32 p = B[t][2] | B[t][3];
    float b0 = bflo(p) * aa[t], b1 = bfhi(p) * aa[t];
    OPQ(b0);
    b0 += DPPF(b0, 0x122); b1 += DPPF(b1, 0x122); b0 += DPPF(b0, 0x124); b1 += DPPF(b1, 0x124); b0 += DPPF(b0, 0x128); b1 += DPPF(b1, 0x128);
    const u32 xa = pk2o(a0, a1), xb = pk2o(b0, b1);
#pragma unroll
    for (int c = 0; c < 4; c++) {
      const bool e0 = (n & 13) == 4 * c, e1 = (n & 13) == 4 * c + 1;
      const v4u gb = {e0 ? xa : 0u, e1 ? xa : 0u, e0 ? xb : 0u, e1 ? xb : 0u};
#pragma unroll
      for (int j = 0; j < 2; j++) R[t][j] = MFMA((v4u{tr[t][2 * c + j][0], tr[t][2 * c + j][1], Y[t][2 * c + j][0], Y[t][2 * c + j][1]}), gb, (c ? R[t][j] : Z4));
    }
  }
#pragma unroll
  for (int t = 0; t < NT; t++) {
    const u32 r0 = pk2(R[t][0][0], R[t][0][1]), r1 = pk2(R[t][0][2], R[t][0][3]), r2 = pk2(R[t][1][0], R[t][1][1]), r3 = pk2(R[t][1][2], R[t][1][3]);
    __builtin_amdgcn_raw_buffer_store_b64(v2u{(n & 2) ? r2 : r0, (n & 2) ? r3 : r1}, __builtin_amdgcn_make_buffer_rsrc(out, 0, NN * 256, 0x00020000), oo + 512 * t, 0, 0);
  }
}
#define P3A 0x123
#define P3B 0x123
#define P3C 0x123

#define K3T 768
__global__ void __launch_bounds__(K3T) k3(const u16* __restrict__ x, const u16* __restrict__ dis, char* __restrict__ ws, u16* __restrict__ out) {
#define KID 3
  __shared__ __attribute__((aligned(16))) char T[163840];
  const int t = threadIdx.x, lane = t & 63, w = __builtin_amdgcn_readfirstlane(t >> 6), slot = blockIdx.x;
  asm volatile("" :: "v"((u32)(size_t)(LAS char*)T));
  STAMP_INIT STAMP(0);
  const v4u srd = {(u32)(size_t)ws, (u32)((size_t)ws >> 32) & 0xffffu, WS_DBG, 0x00020000u};
  v4u xv = {0, 0, 0, 0};
  if (t < 640) xv = *(const v4u*)(x + (size_t)slot * 5120 + t * 8);
  const u32 j0 = w < 8 ? 2 * w : min(w + 8, 19), two = w < 8 ? 2 : 0;
  const u16 dq0 = dis[slot * 40 + 2 * j0 + (lane & 1)], dq1 = dis[slot * 40 + 2 * j0 + two + (lane & 1)];
  for (int id = w; id < 70; id += K3T / 64) {
    const u32 k = id / 10 + 1, rem = id - (k - 1) * 10, u = rem >> 1, h = rem & 1;
    const u32 vo = ((lane >> 2) ^ ((k >> 2) << 1)) * GSTRIDE + ((lane ^ k) & 3) * 16;
    const u32 so = WS_G + k * GBUF + h * 16 * GSTRIDE + (u < 4 ? (u >> 1) * 32768 + 128 * slot + (u & 1) * 64 : 65536 + 64 * slot);
    DMA0(vo, so, chunk(u * 8 + k) + h * 1024);
  }
  for (int id = w; id < 68; id += K3T / 64) DMA0(lane * 16, WS_W + id * 1024, id < 32 ? id * 1024 : (id < 64 ? L_WW + (id - 32) * 1024 : L_WR + (id - 64) * 1024));
  if (t < 640) {
    const u32 node = t >> 4, a = chunk((node >> 3) * 8) + (t & 15) * 128 + ((node >> 1) & 3) * 16 + (node & 1) * 8;
    *(LAS v2u*)(size_t)a = v2u{xv[0], xv[1]}; *(LAS v2u*)(size_t)(a + 64) = v2u{xv[2], xv[3]};
  }
  const TfPre P = tfpre(j0, slot, lane);
  float dv0 = bf2f(dq0), dv1 = bf2f(dq1);
  dv0 = dv0 == 0.f ? 1.f : dv0; dv1 = dv1 == 0.f ? 1.f : dv1; OPQ2(dv0, dv1);
  asm volatile("" ::: "memory");
  __builtin_amdgcn_s_waitcnt(0x0070);
  __builtin_amdgcn_s_barrier();
  asm volatile("" ::: "memory");
  STAMP(1);
  if (w < 4) tf<2, P3A>(P, lane, dv0, dv1, out);
  else if (w < 8) tf<2, P3B>(P, lane, dv0, dv1, out);
  else if (w < 12) tf<1, P3C>(P, lane, dv0, dv1, out);
  STAMP(2);
  STAMP_END;
#undef KID
}
__device__ __host__ __forceinline__ u32 rowof(u32 n) { u32 s = n / 40, l = n - s * 40; return l < 32 ? (l >> 4) * 4096 + 16 * s + (l & 15) : 8192 - 32 + 8 * s + l; }

extern "C" void kernel_launch(void* const* d_in, const int* in_sizes, int n_in, void* d_out, int out_size, void* d_ws, size_t ws_size, hipStream_t stream) {
  const u16* x = (const u16*)d_in[0]; const int* src = (const int*)d_in[1]; const int* dst = (const int*)d_in[2]; const u16* dis = (const u16*)d_in[3];
  const u16* wq = (const u16*)d_in[4]; const u16* wk = (const u16*)d_in[5]; const u16* wv = (const u16*)d_in[6]; const u16* wo = (const u16*)d_in[7]; const u16* wr = (const u16*)d_in[8];
  char* ws = (char*)d_ws;
  k1<<<253, 640, 0, stream>>>(src, dst, x, dis, wq, wk, wv, wo, ws);
  k1b<<<256, 1024, 0, stream>>>(wq, wk, wv, wo, wr, ws);
  k2<<<256, 1024, 0, stream>>>(dis, ws);
  k3<<<250, K3T, 0, stream>>>(x, dis, ws, (u16*)d_out);
}
#define WS_TOK WS_G
#define TOKBYTES (8u * GBUF)
#define TOKSC(n) hbf2f(dis[n])
#define TOKADDR(n,k,d) ((size_t)(k) * GBUF + ((d) >> 2) * GSTRIDE + rowof(n) * 8 + ((d) & 3) * 2)
```

```cpp
#include <hip/hip_runtime.h>
#include <hip/hip_bf16.h>
#include <stdint.h>
#include <type_traits>

#define NN 10000
#define EE 320000
typedef unsigned short u16;
typedef unsigned u32;
typedef unsigned long long u64;
typedef u32 v4u __attribute__((ext_vector_type(4)));
typedef u32 v2u __attribute__((ext_vector_type(2)));
typedef short v4s __attribute__((ext_vector_type(4)));
typedef float v4f __attribute__((ext_vector_type(4)));
typedef __bf16 v4bf __attribute__((ext_vector_type(4)));
typedef __bf16 v8bf __attribute__((ext_vector_type(8)));
typedef __bf16 v16bf __attribute__((ext_vector_type(16)));
typedef __bf16 v2bf __attribute__((ext_vector_type(2)));
typedef float v2f __attribute__((ext_vector_type(2)));
typedef u16 v2h __attribute__((ext_vector_type(2)));
#define LAS __attribute__((address_space(3)))

#define GSTRIDE 81920u
#define GBUF    (32u * GSTRIDE)
#define WS_G    0u
#define WS_SUB  (8u * GBUF)
#define WS_ELL  (WS_SUB + 4096000u)
#define WS_HDR  (WS_ELL + 1280000u)
#define WS_CNT  (WS_HDR + 2048u)
#define WS_OVF1 (WS_CNT + 256u)
#define WS_OVF2 (WS_OVF1 + 128u * 2504u * 4u)
#define WS_W    (WS_OVF2 + 1280000u)
#define WS_DBG  (WS_W + 69632u)
#define POISON 0xFFFFFFFFu
#define OOB 0x40000000u
#define ZROW 15312u
#define MAXSPIN 100000u
#define AUX1 16

#define STAMP_INIT
#define STAMP(i)
#define STAMP_END

#define WST
#define WSA
#define PSTAMP(i) STAMP(i)
__device__ __forceinline__ float bf2f(u16 v) { return __uint_as_float(((u32)v) << 16); }
__device__ __forceinline__ float bflo(u32 v) { return __uint_as_float(v << 16); }
__device__ __forceinline__ float bfhi(u32 v) { return __uint_as_float(v & 0xffff0000u); }
__device__ __forceinline__ u32 pk2(float a, float b) { v2f f = {a, b}; v2bf r = __builtin_convertvector(f, v2bf); return __builtin_bit_cast(u32, r); }
__device__ __forceinline__ u32 pk2s(float a, float b) { return min(pk2(a, b), 0xFFFFFFFEu); }
#define DPPU(v, ctrl) ((u32)__builtin_amdgcn_mov_dpp((int)(v), (ctrl), 0xF, 0xF, true))
#define LBAR() do { asm volatile("" ::: "memory"); __builtin_amdgcn_s_waitcnt(0xC07F); __builtin_amdgcn_s_barrier(); asm volatile("" ::: "memory"); } while (0)
__device__ __forceinline__ u32 tabidx(u32 n) {
  u32 s = (n * 52429u) >> 21, l = n - s * 40u;
  u32 row = l < 32u ? ((l >> 4) << 12) + 16u * s + (l & 15u) : 8192u - 32u + 8u * s + l;
  return row + (row >= 5120u ? 5120u : 0u);
}

#define DMA0(voff, soff, lbase) asm volatile("s_mov_b32 m0, %2\n\ts_nop 0\n\tbuffer_load_dwordx4 %0, %3, %1 offen lds" :: "v"(voff), "s"(soff), "s"(lbase), "s"(srd) : "memory")
__device__ __forceinline__ v4bf rd(u32 a) { return __builtin_amdgcn_ds_read_tr16_b64_v4bf16((LAS v4bf*)(size_t)a); }
#define MFMA(a, b, c) __builtin_amdgcn_mfma_f32_16x16x32_bf16(__builtin_bit_cast(v8bf, (v4u)(a)), __builtin_bit_cast(v8bf, (v4u)(b)), (c), 0, 0, 0)
__device__ __forceinline__ void sw16(float& a, float& b) { const v2u r = __builtin_amdgcn_permlane16_swap(__float_as_uint(a), __float_as_uint(b), false, false); a = __uint_as_float(r[0]); b = __uint_as_float(r[1]); }
__device__ __forceinline__ void sw32(float& a, float& b) { const v2u r = __builtin_amdgcn_permlane32_swap(__float_as_uint(a), __float_as_uint(b), false, false); a = __uint_as_float(r[0]); b = __uint_as_float(r[1]); }
#define DPPF(v, ctrl) __uint_as_float(DPPU(__float_as_uint(v), ctrl))
#define LOG2E 1.4426950408889634f
template <class F>
__device__ __forceinline__ void wprep(const int which, const u16* wq, const u16* wk, const u16* wv, const u16* wo, const u16* wr, char* ws, float* L, F&& early WST) {
  const int t = threadIdx.x, lane = t & 63, m = __builtin_amdgcn_readfirstlane(t >> 6), g = lane >> 4, i = lane & 15;
  v4f D[8];
  v4u B[4];
  const float wra = bf2f(wr[lane]), wrb = bf2f(wr[64 + lane]), wrm = bf2f(wr[16 * (m & 7) + i]);
  const u32 lb = (u32)(size_t)(LAS float*)L;
  if (m < 8) {
    const u16* wa = which == 0 ? wq : wv;
    {
      const v4u srd = {(u32)(size_t)wa, (u32)((size_t)wa >> 32) & 0xffffu, 32768u, 0x00020000u};
#pragma unroll
      for (int ks = 0; ks < 4; ks++) DMA0(i * 256 + g * 16, m * 4096 + ks * 64, lb + (m * 4 + ks) * 1024);
    }
    if (which == 0) {
#pragma unroll
      for (int ks = 0; ks < 4; ks++) B[ks] = *(const v4u*)(wk + (16 * m + i) * 128 + 32 * ks + 8 * g);
    } else {
      const v4u srd = {(u32)(size_t)wo, (u32)((size_t)wo >> 32) & 0xffffu, 32768u, 0x00020000u};
#pragma unroll
      for (int ks = 0; ks < 4; ks++) DMA0(lane * 16, (m * 4 + ks) * 1024, lb + 81920 + (m * 4 + ks) * 1024);
    }
  }
  asm volatile("" ::: "memory");
  __builtin_amdgcn_s_waitcnt(0x0070);
  __builtin_amdgcn_s_barrier();
  asm volatile("" ::: "memory");
  early();
  STAMP(40);
  if (m < 8) {
    if (which == 1) {
#pragma unroll
      for (int ks = 0; ks < 4; ks++) {
        const u32 a = lb + 81920 + (32 * ks + 8 * g + (i >> 2)) * 256 + m * 32 + (i & 3) * 8;
        const v2u lo = __builtin_bit_cast(v2u, rd(a)), hi = __builtin_bit_cast(v2u, rd(a + 1024));
        B[ks] = v4u{lo[0], lo[1], hi[0], hi[1]};
      }
    }
    float cs = 0.f;
#pragma unroll
    for (int dt = 0; dt < 8; dt++) {
      v4f a = {0, 0, 0, 0};
#pragma unroll
      for (int ks = 0; ks < 4; ks++) a = MFMA(*(const LAS v4u*)(size_t)(lb + (dt * 4 + ks) * 1024 + lane * 16), B[ks], a);
      D[dt] = a; cs += (a[0] + a[1]) + (a[2] + a[3]);
    }
    STAMP(41);
    float c2 = cs; sw16(cs, c2); cs += c2; c2 = cs; sw32(cs, c2); cs = (cs + c2) * (1.f / 128.f);
#pragma unroll
    for (int dt = 0; dt < 8; dt++) D[dt] -= cs;
    float wc = 0.f;
    if (which == 1) {
      float z = wra + wrb; z += DPPF(z, 0x121); z += DPPF(z, 0x122); z += DPPF(z, 0x124); z += DPPF(z, 0x128);
      c2 = z; sw16(z, c2); z += c2; c2 = z; sw32(z, c2); z = (z + c2) * (1.f / 128.f);
      wc = (wrm - z) * LOG2E;
      if (g == 0) L[28672 + 16 * m + i] = wc;
    }
#pragma unroll
    for (int pass = 0; pass < 2; pass++) {
      if (pass == 0 || which == 1) {
        v4f r[8];
#pragma unroll
        for (int dt = 0; dt < 8; dt++) r[dt] = pass ? D[dt] * wc : D[dt];
        __builtin_amdgcn_sched_barrier(0);
#pragma unroll
        for (int st = 0; st < 4; st++) {
#pragma unroll
          for (int dt = 0; dt < 8; dt++)
#pragma unroll
            for (int q = 0; q < 4; q++) { const float z = r[dt][q]; r[dt][q] = z + (st == 0 ? DPPF(z, 0x121) : st == 1 ? DPPF(z, 0x122) : st == 2 ? DPPF(z, 0x124) : DPPF(z, 0x128)); }
          __builtin_amdgcn_sched_barrier(0);
        }
        if (i == 0) {
#pragma unroll
          for (int dt = 0; dt < 8; dt++) *(v4f*)(L + 8192 + pass * 1024 + m * 128 + dt * 16 + 4 * g) = r[dt];
        }
      }
    }
  }
  STAMP(42);
  LBAR();
  if (t < 256) {
    float z = 0.f;
#pragma unroll
    for (int q = 0; q < 8; q++) z += L[8192 + (t >> 7) * 1024 + q * 128 + (t & 127)];
    L[t] = z;
  }
  LBAR();
  if (which == 0) {
    if (m < 8) {
#pragma unroll
      for (int dt = 0; dt < 8; dt++) D[dt] = (D[dt] - *(const v4f*)(L + dt * 16 + 4 * g) * (1.f / 128.f)) * (LOG2E * 0.08838834764831845f);
    }
  } else if (m < 4) {
    const float* p = L + ((i & 3) == 1 ? 28672 : ((i & 1) ? 128 : 0)) + 32 * m + 4 * g;
    v4f a = *(const v4f*)p, c = *(const v4f*)(p + 16);
    v4u o = {pk2(a[0], a[1]), pk2(a[2], a[3]), pk2(c[0], c[1]), pk2(c[2], c[3])};
    if ((i & 7) == 5) o = v4u{pk2(a[0] - bflo(o[0]), a[1] - bfhi(o[0])), pk2(a[2] - bflo(o[1]), a[3] - bfhi(o[1])), pk2(c[0] - bflo(o[2]), c[1] - bfhi(o[2])), pk2(c[2] - bflo(o[3]), c[3] - bfhi(o[3]))};
    if ((i & 3) == 0) o = v4u{0x3f803f80u, 0x3f803f80u, 0x3f803f80u, 0x3f803f80u};
    *(v4u*)(ws + WS_W + 65536 + m * 1024 + lane * 16) = o;
  }
  STAMP(43);
  if (m < 8) {
#pragma unroll
    for (int s = 0; s < 4; s++) {
      const v4u o = {pk2(D[2 * s][0], D[2 * s][1]), pk2(D[2 * s][2], D[2 * s][3]), pk2(D[2 * s + 1][0], D[2 * s + 1][1]), pk2(D[2 * s + 1][2], D[2 * s + 1][3])};
      *(v4u*)(ws + WS_W + which * 32768 + (m * 4 + s) * 1024 + lane * 16) = o;
    }
  }
}

__global__ void __launch_bounds__(640) k1(const int* __restrict__ src, const int* __restrict__ dst, const u16* __restrict__ x,
                                          const u16* __restrict__ dis, const u16* __restrict__ wq, const u16* __restrict__ wk,
                                          const u16* __restrict__ wv, const u16* __restrict__ wo, char* __restrict__ ws) {
#define KID 0
  __shared__ __attribute__((aligned(16))) u32 L[8704];
  const int t = threadIdx.x, b = blockIdx.x;
  const __amdgpu_buffer_rsrc_t rs = __builtin_amdgcn_make_buffer_rsrc(ws, 0, WS_DBG, 0x00020000);
  STAMP_INIT STAMP(0);
  if (b < 128) {
    u32* stage = L; u32* cnt = L + 8000; u32* novf = L + 8256;
    v4u s4 = {0, 0, 0, 0}, d4 = {0, 0, 0, 0};
    if (t < 625) { s4 = *(const v4u*)(src + b * 2500 + t * 4); d4 = *(const v4u*)(dst + b * 2500 + t * 4); }
    u32 pf = 0;
    if (t >= 632) pf = *(const u32*)(((b & 64) ? ((b & 32) ? wo : wv) : ((b & 32) ? wk : wq)) + (b & 31) * 512 + (t - 632) * 64);
    if (t < 257) cnt[t] = 0;
    if (b == 0 && t == 0) *(u32*)(ws + WS_CNT) = 0;
    __syncthreads();
    STAMP(1);
    u32* ovf = (u32*)(ws + WS_OVF1) + b * 2504;
    if (t < 625) {
      u32 r[4], bin[4], en[4];
#pragma unroll
      for (int i = 0; i < 4; i++) {
        u32 d = d4[i];
        bin[i] = (d * 52429u) >> 21;
        r[i] = atomicAdd(&cnt[bin[i]], 1u);
        en[i] = tabidx(s4[i]) | ((d - bin[i] * 40u) << 16);
      }
#pragma unroll
      for (int i = 0; i < 4; i++) {
        if (r[i] < 31) stage[bin[i] * 32 + 1 + r[i]] = en[i];
        else { u32 o = atomicAdd(novf, 1u); ovf[4 + o] = (en[i] & 0xffffu) | (d4[i] << 16); }
      }
    }
    __syncthreads();
    STAMP(2);
    if (t == 0) ovf[0] = *novf;
    asm volatile("" :: "v"(pf));
#pragma unroll
    for (int it = 0; it < 4; it++) {
      int i = t + it * 640;
      if (i < 2000) {
        int bin = i >> 3, q = i & 7;
        u32 c = cnt[bin];
        if ((u32)(q * 4) <= c) {
          v4u v = *(const v4u*)(stage + bin * 32 + q * 4);
          if (q == 0) v[0] = c;
          __builtin_amdgcn_raw_buffer_store_b128(v, rs, WS_SUB + b * 128 + bin * 16384 + q * 16, 0, AUX1);
        }
      }
    }
    STAMP(3);
  } else {
    const int wb = b - 128;
    char* S = (char*)L;
    v4u v[2]; float dv[2]; u32 go[2];
#pragma unroll
    for (int it = 0; it < 2; it++) {
      int i = t + it * 640; int node = i >> 4, cq = i & 15;
      v[it] = *(const v4u*)(x + (size_t)(wb * 80 + node) * 128 + cq * 8);
      dv[it] = bf2f(dis[wb * 80 + node]);
    }
#pragma unroll
    for (int it = 0; it < 2; it++) {
      int i = t + it * 640; int g = i / 40, pc = i - g * 40;
      go[it] = g * GSTRIDE + (pc < 32 ? (pc >> 4) * 32768 + wb * 256 + (pc & 15) * 16 : 65536 + wb * 128 + (pc - 32) * 16);
    }
#pragma unroll
    for (int it = 0; it < 2; it++) {
      int i = t + it * 640; int node = i >> 4, cq = i & 15;
      int s = node >= 40, l = node - s * 40;
      int pos = l < 32 ? (l >> 4) * 32 + s * 16 + (l & 15) : 32 + s * 8 + l;
      v2u a, c;
      a[0] = pk2s(bflo(v[it][0]) * dv[it], bfhi(v[it][0]) * dv[it]);
      a[1] = pk2s(bflo(v[it][1]) * dv[it], bfhi(v[it][1]) * dv[it]);
      c[0] = pk2s(bflo(v[it][2]) * dv[it], bfhi(v[it][2]) * dv[it]);
      c[1] = pk2s(bflo(v[it][3]) * dv[it], bfhi(v[it][3]) * dv[it]);
      *(v2u*)(S + (2 * cq) * 656 + pos * 8) = a;
      *(v2u*)(S + (2 * cq + 1) * 656 + pos * 8) = c;
    }
    __syncthreads();
    STAMP(2);
#pragma unroll
    for (int it = 0; it < 2; it++) {
      int i = t + it * 640; int g = i / 40, pc = i - g * 40;
      v4u o = *(const v4u*)(S + g * 656 + pc * 16);
      __builtin_amdgcn_raw_buffer_store_b128(o, rs, WS_G + go[it], 0, AUX1);
    }
    const v4u pz = {POISON, POISON, POISON, POISON};
#pragma unroll
    for (int it = 0; it < 4; it++) __builtin_amdgcn_raw_buffer_store_b128(pz, rs, WS_G + go[it & 1], (1 + (it >> 1)) * GBUF, 16);
    __builtin_amdgcn_raw_buffer_store_b128(pz, rs, WS_ELL + wb * 10240 + t * 16, 0, 16);
    if (t == 0) __builtin_amdgcn_raw_buffer_store_b128(pz, rs, WS_HDR + wb * 16, 0, 16);
    STAMP(3);
  }
  STAMP_END;
#undef KID
}

#define B_ELL 0
#define B_CB 1280
#define B_CN 1472
#define B_BUSY 2240
#define B_FULL 2432
#define B_ROWM 2480
#define B_NT 2576
#define B_NF 2577
#define B_Z0 1280
#define B_ZN 1304
#define B_Q 2592
#define B_F 20480
#define B_N1 2578
#define B_NM 2579
#define B_NO 2580
#define B_M2 2581
#define B_BASE 2582
template <class F>
__device__ __forceinline__ void build(const int slot, char* __restrict__ ws, u32* L, F&& early WST) {
  const int t = threadIdx.x, lane = t & 63, w = __builtin_amdgcn_readfirstlane(t >> 6);
  const __amdgpu_buffer_rsrc_t rs = __builtin_amdgcn_make_buffer_rsrc(ws, 0, WS_DBG, 0x00020000);
  v4u v; u32 n1;
  {
    const v4u srd = {(u32)(size_t)ws, (u32)((size_t)ws >> 32) & 0xffffu, WS_DBG, 0x00020000u};
    asm volatile("buffer_load_dwordx4 %0, %2, %4, %5 offen\n\tbuffer_load_dword %1, %3, %4, 0 offen"
                 : "=&v"(v), "=&v"(n1) : "v"(t * 16), "v"(t < 128 ? WS_OVF1 + t * 10016u : OOB), "s"(srd), "s"(WS_SUB + slot * 16384) : "memory");
  }
  if (t < 320) { const v4u z = {POISON, POISON, POISON, POISON}; *(v4u*)(L + B_ELL + t * 4) = z; }
  else if (t < 320 + B_ZN / 4) { const v4u z = {0, 0, 0, 0}; *(v4u*)(L + B_Z0 + (t - 320) * 4) = z; }
  LBAR();
  early();
  asm volatile("s_waitcnt vmcnt(5)" : "+v"(v), "+v"(n1) :: "memory");
  STAMP(40);
  {
    const u32 q = t & 7;
    u32 c = DPPU(v[0], 0x00);
    c = (u32)__builtin_amdgcn_update_dpp((int)c, (int)c, 0x114, 0xF, 0xA, false);
    c = min(c, 31u);
    u32 inc = c + (u32)__builtin_amdgcn_update_dpp(0, (int)c, 0x118, 0xF, 0xC, false);
    inc += (u32)__builtin_amdgcn_update_dpp(0, (int)inc, 0x142, 0xA, 0xF, false);
    inc += (u32)__builtin_amdgcn_update_dpp(0, (int)inc, 0x143, 0xC, 0xF, false);
    u32 base = 0;
    if (lane == 63) base = atomicAdd(&L[B_NT], inc);
    base = __builtin_amdgcn_readlane(base, 63);
    const u32 pos = B_Q + base + inc - c + 4 * q - 1;
#pragma unroll
    for (int i = 0; i < 4; i++) {
      const u32 id = 4 * q + i;
      L[(id - 1u < c) ? pos + i : B_Q + 4096 + lane] = v[i];
    }
  }
  if (n1) atomicAdd(&L[B_N1], n1);
  LBAR();
  STAMP(41);
  auto place = [&](u32 e, u32 r, u32 c) {
    const u32 loc = e >> 16, hp = loc >> 3;
    const u32 j = hp == 4 ? 24 + (r >> 1) : (r < 12 ? (hp >> 1) * 12 + r : 20 + (hp >> 1) * 4 + r);
    const u32 half = (hp == 4 ? r : hp) & 1;
    const u32 ln = half * 32 + (loc & 4) * 4 + c * 4 + (loc & 3);
    ((u16*)(L + B_ELL))[(j >> 3) * 512 + ln * 8 + (j & 7)] = (u16)e;
    atomicOr(&L[B_ROWM + half * 40 + j], 1u << ((e + 16) & 31));
  };
  const u32 tot = L[B_NT];
  for (u32 b0 = w * 128; b0 < tot; b0 += 2048) {
    u32 e[2], hb[2], rk[2], r[2], c[2];
#pragma unroll
    for (int i = 0; i < 2; i++) { const u32 id = b0 + i * 64 + lane; e[i] = L[B_Q + id]; if (id >= tot) e[i] = ((40 + (lane & 7)) << 16) | (lane & 31); }
#pragma unroll
    for (int i = 0; i < 2; i++) { hb[i] = (e[i] >> 19) * 32 + (e[i] & 31); rk[i] = atomicAdd(&L[B_CB + hb[i]], 1u); }
#pragma unroll
    for (int i = 0; i < 2; i++) {
      r[i] = e[i] >= (32u << 16) ? (rk[i] + (e[i] >> 1)) & 15 : (rk[i] < 12 ? rk[i] : rk[i] - 12);
      c[i] = atomicAdd(&L[rk[i] < 24 ? B_CN + (e[i] >> 16) * 16 + r[i] : B_CN + 640 + lane], 1u);
    }
#pragma unroll
    for (int i = 0; i < 2; i++) {
      if (e[i] < (40u << 16)) {
        if (rk[i] < 24 && c[i] < 4) {
          place(e[i], r[i], c[i]);
          atomicOr(&L[B_BUSY + hb[i]], 1u << r[i]);
          if (c[i] == 3) atomicOr(&L[B_FULL + (e[i] >> 16)], 1u << r[i]);
        } else L[B_F + atomicAdd(&L[B_NF], 1u)] = e[i];
      }
    }
  }
  LBAR();
  STAMP(42);
  const u32 nf = L[B_NF];
  for (u32 id = t; id < nf; id += 1024) {
    const u32 e = L[B_F + id], loc = e >> 16, hb = (e >> 19) * 32 + (e & 31);
    u32 rho = (id * 5 + (id >> 2)) & 15, tried = 0;
    const u32 pm = loc >= 32 ? 0xFFFFu : 0xFFFu;
    for (int round = 0;; round++) {
      const u32 bs = __hip_atomic_load(&L[B_BUSY + hb], __ATOMIC_RELAXED, __HIP_MEMORY_SCOPE_WORKGROUP);
      const u32 nfl = ~(__hip_atomic_load(&L[B_FULL + loc], __ATOMIC_RELAXED, __HIP_MEMORY_SCOPE_WORKGROUP) | tried);
      u32 cand = nfl & ~bs & pm;
      const bool strict = cand != 0 && round < 2;
      if (!cand) cand = nfl & pm;
      if (!cand) cand = nfl & ~bs & 0xF000;
      if (!cand) cand = nfl & 0xF000;
      if (!cand) { L[B_Q + atomicAdd(&L[B_NO], 1u)] = (e & 0xffffu) | ((slot * 40 + loc) << 16); break; }
      const u32 r = (rho + __builtin_ctz((cand | (cand << 16)) >> rho)) & 15;
      const u32 ob = atomicOr(&L[B_BUSY + hb], 1u << r);
      const u32 c = atomicAdd(&L[B_CN + loc * 16 + r], 1u);
      if (c == 3) atomicOr(&L[B_FULL + loc], 1u << r);
      if (c < 4 && !(strict && ((ob >> r) & 1))) { place(e, r, c); break; }
      if (c >= 3) tried |= 1u << r;
      rho = (rho + 7) & 15;
    }
  }
  auto scan = [&](auto&& fn) {
    for (int j = 0; j < 128; j++) {
      const u32* o1 = (const u32*)(ws + WS_OVF1) + j * 2504;
      const u32 n = o1[0];
      for (u32 i = t; i < n; i += 1024) { const u32 en = o1[4 + i]; if ((en >> 16) - slot * 40u < 40u) fn(en); }
    }
  };
  const bool sp = L[B_N1] != 0;
  if (sp) scan([&](u32) { atomicAdd(&L[B_NM], 1u); });
  LBAR();
  STAMP(43);
  const u32 no = L[B_NO], nt = no + L[B_NM];
  u32 base = 0;
  if (nt) {
    if (t == 0) L[B_BASE] = atomicAdd((u32*)(ws + WS_CNT), nt);
    LBAR();
    base = L[B_BASE];
    for (u32 i = t; i < no; i += 1024) __builtin_amdgcn_raw_buffer_store_b32(L[B_Q + i], rs, WS_OVF2 + (base + i) * 4, 0, 16);
    if (sp) scan([&](u32 en) { __builtin_amdgcn_raw_buffer_store_b32(en, rs, WS_OVF2 + (base + no + atomicAdd(&L[B_M2], 1u)) * 4, 0, 16); });
    asm volatile("s_waitcnt vmcnt(0)" ::: "memory");
    LBAR();
  }
  if (t < 320) {
    v4u o = *(const v4u*)(L + B_ELL + t * 4);
    const u32* rm = L + B_ROWM + (lane >> 5) * 40 + w * 8;
    const v4u m0 = *(const v4u*)rm, m1 = *(const v4u*)(rm + 4);
#pragma unroll
    for (int i = 0; i < 4; i++) {
      const u32 a = i < 2 ? m0[2 * i] : m1[2 * i - 4], bb = i < 2 ? m0[2 * i + 1] : m1[2 * i - 3];
      u32 fa, fb;
      asm("v_ffbl_b32 %0, %1" : "=v"(fa) : "v"(~a)); asm("v_ffbl_b32 %0, %1" : "=v"(fb) : "v"(~bb));
      v2h d = {(u16)(fa + ZROW), (u16)(fb + ZROW)};
      const u32 oi = o[i];
      o[i] = __builtin_bit_cast(u32, __builtin_elementwise_min(__builtin_bit_cast(v2h, oi), d));
    }
    __builtin_amdgcn_raw_buffer_store_b128(o, rs, WS_ELL + slot * 5120 + t * 16, 0, 16);
  }
  STAMP(5);
  if (t == 320) __builtin_amdgcn_raw_buffer_store_b64(v2u{base, nt}, rs, WS_HDR + slot * 8, 0, 16);
}

__device__ __forceinline__ v16bf rd4(u32 a0, u32 a1, u32 a2, u32 a3) {
  const v4bf a = rd(a0), b = rd(a1), c = rd(a2), d = rd(a3);
  return __builtin_shufflevector(__builtin_shufflevector(a, b, 0, 1, 2, 3, 4, 5, 6, 7), __builtin_shufflevector(c, d, 0, 1, 2, 3, 4, 5, 6, 7),
                                 0, 1, 2, 3, 4, 5, 6, 7, 8, 9, 10, 11, 12, 13, 14, 15);
}
#define DMA(voff, soff, lbase) asm volatile("s_mov_b32 m0, %2\n\ts_nop 0\n\tbuffer_load_dwordx4 %0, %3, %1 offen sc1 lds" :: "v"(voff), "s"(soff), "s"(lbase), "s"(srd) : "memory")
#define PFM 1
#define PR1 0
#define PR2 2
#define PR3 4
#define NPRE 1
#define KMAX 7
#define LASTP 6
#define D23POS 0
#define D4POS 2
#define T2PRE
__global__ void __launch_bounds__(1024) k2(const u16* __restrict__ dis, const u16* __restrict__ wq, const u16* __restrict__ wk, const u16* __restrict__ wv,
                                           const u16* __restrict__ wo, const u16* __restrict__ wr, char* __restrict__ ws) {
#define KID 2
  __shared__ __attribute__((aligned(16))) char T[163840];
  const int t = threadIdx.x, lane = t & 63, w = __builtin_amdgcn_readfirstlane(t >> 6);
  const int b = blockIdx.x, gp = (b & 7) * 2 + (b >> 7), p = (b >> 3) & 15;
  const int slot = p * 16 + w;
  const bool act = slot < 250;
  asm volatile("" :: "v"((u32)(size_t)(LAS char*)T));
  STAMP_INIT STAMP(0);
  const __amdgpu_buffer_rsrc_t rs = __builtin_amdgcn_make_buffer_rsrc(ws + WS_G, 0, 8u * GBUF, 0x00020000);
  const v4u srd = {(u32)(size_t)(ws + WS_G), (u32)((size_t)(ws + WS_G) >> 32) & 0xffffu, 8u * GBUF, 0x00020000u};
  const u32 sgA = gp * 2 * GSTRIDE;
  const u32 vla = w * 1024 + lane * 16;
  const u32 vlc = (w == 15 && lane >= 40) ? OOB : vla;
  const u32 lb0 = w * 1024, lb2 = w * 1024 + 32768 + (w >= 8 ? 40960 : 0);
  auto early = [&]() __attribute__((always_inline)) {
    DMA(vla, sgA, lb0); DMA(vla, sgA + 16384, lb0 + 16384); DMA(vla, sgA + 32768, lb2); DMA(vla, sgA + 49152, lb0 + 90112); DMA(vlc, sgA + 65536, lb0 + 106496);
  };
  {
    const int bs = p * 16 + gp;
    if (bs < 250) build(bs, ws, (u32*)(T + 40960), early WSA);
    else {
      if (bs < 252) wprep(bs - 250, wq, wk, wv, wo, wr, ws, (float*)(T + 40960), early WSA);
      else early();
    }
  }
  STAMP(44);
  v4u e[5]; u16 dvr[3];
  const int sl = act ? slot : 0;
  dvr[0] = dis[sl * 40 + (lane >> 2)]; dvr[1] = dis[sl * 40 + 16 + (lane >> 2)]; dvr[2] = dis[sl * 40 + 32 + ((lane & 31) >> 2)];
  const __amdgpu_buffer_rsrc_t rw = __builtin_amdgcn_make_buffer_rsrc(ws + WS_ELL, 0, WS_W - WS_ELL, 0x00020000);
  v2u hd;
  for (u32 spins = 0;; spins++) {
#pragma unroll
    for (int i = 0; i < 5; i++) e[i] = __builtin_amdgcn_raw_buffer_load_b128(rw, lane * 16 + i * 1024, sl * 5120, 16);
    hd = __builtin_amdgcn_raw_buffer_load_b64(rw, 0, WS_HDR - WS_ELL + sl * 8, 16);
    u32 m = max(hd[0], hd[1]);
#pragma unroll
    for (int i = 0; i < 5; i++) m = max(max(m, max(e[i][0], e[i][1])), max(e[i][2], e[i][3]));
    if (__builtin_amdgcn_ballot_w64(m == POISON) == 0 || spins >= MAXSPIN) break;
    __builtin_amdgcn_s_sleep(4);
    asm volatile("" ::: "memory");
  }
  const u32 novf = act ? min((u32)__builtin_amdgcn_readfirstlane(hd[1]), (u32)EE) : 0u;
  const u32 ovb = WS_OVF2 - WS_ELL + min((u32)__builtin_amdgcn_readfirstlane(hd[0]), (u32)EE) * 4;
  STAMP(45);
  const u32 vp01 = act ? slot * 128 + (lane >> 1) * 4 : OOB;
  const u32 vp2 = act ? 65536 + slot * 64 + (lane >> 1) * 4 : OOB;
  v8bf sel1, sel2; const int seli = (lane & 1) ? 0xEEEEEEEE : 0x44444444;
  {
    const u32 on = (lane & 2) ? 0u : 0x3f803f80u, lo = (lane & 4) ? 0u : on, hi = (lane & 4) ? on : 0u;
    const bool m1 = ((lane >> 4) & 1) == ((lane >> 3) & 1);
    const v4u a2 = {lo, lo, hi, hi}, a1 = {m1 ? lo : 0u, m1 ? lo : 0u, m1 ? hi : 0u, m1 ? hi : 0u};
    sel1 = __builtin_bit_cast(v8bf, a1); sel2 = __builtin_bit_cast(v8bf, a2);
  }
  const u32 vpz = w < 10 ? (w >= 5 ? GSTRIDE : 0u) + ((w % 5) >> 1) * 32768 + p * (w % 5 < 4 ? 2048 : 1024) + ((w % 5) & 1) * 1024 + lane * 16 : OOB;
  u32 vchk;
  {
    const u32 i = (lane >> 3) & 3, ln = (w == 15 && (i & 1)) ? (lane & 1) : (lane & 7);
    const u32 hl = (w == 15 && (lane & 15) >= 10) ? (lane & 7) : (lane & 15);
    vchk = lane < 32 ? (i == 2 ? lb2 : lb0 + (i == 1 ? 16384 : (i == 3 ? 90112 : 0))) + ln * 128 + lane * 4 : lb0 + 106496 + hl * 64 + (lane & 15) * 4;
  }
  if (t < 32) { const v4u z = {0, 0, 0, 0}; *(LAS v4u*)(size_t)(ZROW * 8 + (t >> 4) * 40960 + (t & 15) * 16) = z; }
  u32 ad[32];
#pragma unroll
  for (int i = 0; i < 16; i++) { const u32 q = e[i >> 2][i & 3]; ad[2 * i] = (q & 0xffffu) * 8; ad[2 * i + 1] = (q >> 16) * 8; }
  u32 ex[4] = {e[4][0], e[4][1], e[4][2], e[4][3]};
  bool xm;
  {
    const u32 m0 = min(min(ex[0] & 0xffffu, ex[0] >> 16), min(ex[1] & 0xffffu, ex[1] >> 16));
    const u32 m1 = min(min(ex[2] & 0xffffu, ex[2] >> 16), min(ex[3] & 0xffffu, ex[3] >> 16));
    xm = __builtin_amdgcn_ballot_w64(min(m0, m1) < ZROW) != 0;
  }
  float dv2[3];
#pragma unroll
  for (int i = 0; i < 3; i++) { const float d = bf2f(dvr[i]); dv2[i] = d * d; }
  asm volatile("" ::: "memory");
  __builtin_amdgcn_s_waitcnt(0x0070);
  __builtin_amdgcn_s_barrier();
  asm volatile("" ::: "memory");
  STAMP(1);

  auto slow = [&](const u32 tofs, const u32 n0, const u32 nn) __attribute__((always_inline)) -> float {
    float a = 0.f;
    for (u32 base = 0; base < novf; base += 64) {
      const u32 en = base + lane < novf ? __builtin_amdgcn_raw_buffer_load_b32(rw, (base + lane) * 4, ovb, 16) : POISON;
      u64 m = __builtin_amdgcn_ballot_w64((en >> 16) - n0 < nn);
      while (m) {
        const int l = __builtin_ctzll(m); m &= m - 1;
        const u32 es = __builtin_amdgcn_readlane(en, l);
        const float val = bf2f(*(LAS u16*)(size_t)((es & 0xffffu) * 8 + tofs + (lane & 3) * 2));
        a += ((es >> 16) - n0 == (u32)(lane >> 2)) ? val : 0.f;
      }
    }
    return a;
  };
  auto grow = [&](const u32 la, const u32 scur) __attribute__((always_inline)) -> v2u {
    v2u v = {0, 0};
    if (la < ZROW * 8) {
      u32 spins = 0;
      do {
        v = __builtin_amdgcn_raw_buffer_load_b64(rs, la >= 81920 ? la - 40960 : la, scur, 16);
        asm volatile("" ::: "memory");
      } while (max(v[0], v[1]) == POISON && ++spins < MAXSPIN);
    }
    return v;
  };
  auto fallback = [&](const int bt, const u32 scur) __attribute__((always_inline)) -> float {
    float s0 = 0.f, s1 = 0.f, s2 = 0.f, s3 = 0.f;
#pragma unroll 1
    for (int j = 0; j < (bt < 2 ? 16 : 8); j++) {
      const int je = j < 12 ? bt * 12 + j : 20 + bt * 4 + j;
      const u32 la = __builtin_amdgcn_raw_buffer_load_b16(rw, (je >> 3) * 1024 + lane * 16 + (je & 7) * 2, sl * 5120, 16) * 8u;
      const v2u v = grow(la, scur);
      s0 += bflo(v[0]); s1 += bfhi(v[0]); s2 += bflo(v[1]); s3 += bfhi(v[1]);
    }
    float a = 0.f;
#pragma unroll
    for (int c = 0; c < 4; c++) {
      const int sl_ = (lane & 48) + 4 * c + ((lane >> 2) & 3);
      const float t0 = __shfl(s0, sl_), t1 = __shfl(s1, sl_), t2 = __shfl(s2, sl_), t3 = __shfl(s3, sl_);
      a += (lane & 2) ? ((lane & 1) ? t3 : t2) : ((lane & 1) ? t1 : t0);
    }
    if (bt == 2) a += __shfl_xor(a, 32);
    const u32 n0 = slot * 40 + bt * 16;
    for (u32 i = 0; i < novf; i++) {
      const u32 es = __builtin_amdgcn_raw_buffer_load_b32(rw, i * 4, ovb, 16);
      if ((es >> 16) - n0 == (u32)(lane >> 2) && (bt < 2 || lane < 32)) {
        const v2u v = grow((es & 0xffffu) * 8, scur);
        const u32 q = (lane & 2) ? v[1] : v[0];
        a += (lane & 1) ? bfhi(q) : bflo(q);
      }
    }
    return a;
  };
  auto tail = [&](const int bt, const v4f acc, const u32 scur, const u32 voff, const u32 soff) __attribute__((always_inline)) {
    float a = acc[0] + acc[1];
    if (__builtin_expect(__builtin_amdgcn_ballot_w64(a != a) != 0, 0)) a = fallback(bt, scur);
    const float d2 = dv2[bt];
    const float tn = __builtin_bit_cast(float, DPPU(__float_as_uint(a), 0xB1)) * d2;
    const u32 cm = min(pk2(a * d2, tn), 0xFFFFFFFEu);
    asm volatile("s_mov_b64 exec, %4\n\tbuffer_store_dword %0, %1, %2, %3 offen sc1\n\ts_mov_b64 exec, -1"
                 :: "v"(cm), "v"(voff), "s"(srd), "s"(soff), "s"(bt < 2 ? 0x5555555555555555ull : 0x55555555ull) : "memory");
  };
  v4f acc = {0, 0, 0, 0}, acc2 = {0, 0, 0, 0};
  u32 vp2x = OOB;
  auto phase = [&](auto GBc, auto XMc, const bool recv, const u32 sprev, const u32 scur, const u32 srecv_, const int sb) __attribute__((always_inline)) {
    const u32 srecv = srecv_;
    constexpr u32 GB = decltype(GBc)::value;
    constexpr bool XM = decltype(XMc)::value;
    constexpr u32 to = GB * 40960u, ro = (1 - GB) * 40960u;
    constexpr int NQ = XM ? 10 : 8, E0 = XM ? 3 : 2, E1 = XM ? 7 : 5;
    __builtin_amdgcn_s_setprio(3);
    if (recv) { DMA(vla, srecv, lb0 + ro); DMA(vla, srecv + 16384, lb0 + 16384 + ro); }
    auto grp = [&](const int s) __attribute__((always_inline)) -> v16bf {
      if (XM && (s == E0 || s == E1)) {
        u32 q0 = ex[s == E0 ? 0 : 2], q1 = ex[s == E0 ? 1 : 3];
        asm volatile("" : "+v"(q0), "+v"(q1));
        return rd4((q0 & 0xffffu) * 8 + to, (q0 >> 16) * 8 + to, (q1 & 0xffffu) * 8 + to, (q1 >> 16) * 8 + to);
      }
      const int q = XM ? s - (s > E0) - (s > E1) : s;
      return rd4(ad[4 * q] + to, ad[4 * q + 1] + to, ad[4 * q + 2] + to, ad[4 * q + 3] + to);
    };
    v16bf g[3];
#pragma unroll
    for (int q = 0; q < 3; q++) g[q] = grp(q);
    __builtin_amdgcn_sched_barrier(0);
    if (D23POS < 0 && recv) { DMA(vla, srecv + 32768, lb2 + ro); DMA(vla, srecv + 49152, lb0 + 90112 + ro); }
    float e0 = 0.f, e1 = 0.f, e2 = 0.f;
    if (__builtin_expect(novf != 0, 0)) { e0 = slow(to, slot * 40, 16); e1 = slow(to, slot * 40 + 16, 16); e2 = slow(to, slot * 40 + 32, 8); }
    __builtin_amdgcn_sched_barrier(0);
    u32 chk = 0;
#pragma unroll
    for (int q = 0; q < NQ; q++) {
      if (q == 0) { acc[0] = e0; acc[1] = 0.f; }
      if (q == E0 + 1) { acc[0] = e1; acc[1] = 0.f; }
      if (q == E1 + 1) { acc2[0] = e2; acc2[1] = 0.f; }
      if (q == NQ - 1 && recv) {
        asm volatile("s_waitcnt vmcnt(%0)" :: "n"(D4POS <= E0 ? 2 : 1) : "memory");
        chk = *(volatile LAS u32*)(size_t)(vchk + ro);
      }
      if (q <= E1) acc = __builtin_amdgcn_smfmac_f32_16x16x64_bf16(sel1, g[q % 3], acc, seli, 0, 0);
      else acc2 = __builtin_amdgcn_smfmac_f32_16x16x64_bf16(sel2, g[q % 3], acc2, seli, 0, 0);
      if (q + 3 < NQ) g[q % 3] = grp(q + 3);
      if (q == D23POS && recv) { DMA(vla, srecv + 32768, lb2 + ro); DMA(vla, srecv + 49152, lb0 + 90112 + ro); }
      if (q == D4POS && recv) DMA(vlc, srecv + 65536, lb0 + 106496 + ro);
      if (q == PR1) __builtin_amdgcn_s_setprio(2);
      if (q == PR2) __builtin_amdgcn_s_setprio(1);
      if (q == PR3) __builtin_amdgcn_s_setprio(0);
      __builtin_amdgcn_sched_barrier(0);
      if (q == E0 || q == E1) {
        tail(q == E1, acc, scur, vp01, scur + GBUF + (q == E1 ? 32768 : 0));
        __builtin_amdgcn_sched_barrier(0);
      }
    }
    tail(2, acc2, scur, vp2, scur + GBUF);
    if (GB == 0 && scur < sgA + (LASTP - 2) * GBUF) {
      const v4u pz = {POISON, POISON, POISON, POISON};
      asm volatile("buffer_store_dwordx4 %0, %1, %2, %3 offen sc1" :: "v"(pz), "v"(vpz), "s"(srd), "s"(scur + 3 * GBUF) : "memory");
    }

    PSTAMP(sb);
    if (recv) {
      u32 spins = 0;
      if (__builtin_expect(__builtin_amdgcn_ballot_w64(chk == POISON) != 0, 0)) {
        do {
          spins++;
          DMA(vla, srecv, lb0 + ro); DMA(vla, srecv + 16384, lb0 + 16384 + ro);
          DMA(vla, srecv + 32768, lb2 + ro); DMA(vla, srecv + 49152, lb0 + 90112 + ro);
          DMA(vlc, srecv + 65536, lb0 + 106496 + ro);
          asm volatile("s_waitcnt vmcnt(0)" ::: "memory");
        } while (__builtin_amdgcn_ballot_w64(*(volatile LAS u32*)(size_t)(vchk + ro) == POISON) != 0 && spins < MAXSPIN);
      }
      PSTAMP(sb + 1);
      asm volatile("" ::: "memory");
      __builtin_amdgcn_s_barrier();
      asm volatile("" ::: "memory");
      PSTAMP(sb + 2);
    }
  };
  using F0 = std::integral_constant<u32, 0>; using F1 = std::integral_constant<u32, 1>;
  auto hops = [&](auto XMc) __attribute__((always_inline)) {
    u32 sk = sgA;
#pragma unroll 1
    for (int k = 1; k <= KMAX; k++) {
      phase(F0{}, XMc, true, sk - GBUF + GSTRIDE, sk, sk + GSTRIDE, 6 * k - 4);
      phase(F1{}, XMc, k < KMAX, sk, sk + GSTRIDE, sk + GBUF, 6 * k - 1);
      sk += GBUF;
    }
  };
  if (xm) hops(std::true_type{}); else hops(std::false_type{});
  STAMP(47);
  STAMP_END;
#undef KID
}

#define L_WR 32768u
#define L_WW 81920u
__device__ __forceinline__ u32 chunk(u32 id) { return 40960u + id * 2048u + (id >= 20u ? 40960u : 0u); }
__device__ __forceinline__ v4u ld128(u32 a) { return *(const LAS v4u*)(size_t)a; }
__device__ __forceinline__ v2u ld64(u32 a) { return *(const LAS v2u*)(size_t)a; }
__device__ __forceinline__ float bperm(u32 a, float v) { return __uint_as_float((u32)__builtin_amdgcn_ds_bpermute((int)a, (int)__float_as_uint(v))); }
__device__ __forceinline__ float dot2(u32 a, u32 b, float c) { return __builtin_amdgcn_fdot2_f32_bf16(__builtin_bit_cast(v2bf, a), __builtin_bit_cast(v2bf, b), c, false); }
#define RD 3
#define SB() __builtin_amdgcn_sched_barrier(0)
template <int I, int E, class F> __device__ __forceinline__ void sfor(F&& fn) {
  if constexpr (I < E) { fn(std::integral_constant<int, I>{}); sfor<I + 1, E>(fn); }
}
#define fmx(a, b) __builtin_amdgcn_fmed3f((a), (b), finf)
#define PS1 24
#define PS2 48
#define FO(mm) ((mm) == 0 ? 39 : 38 + 4 * (mm) - ((mm) == 7))
#define OPQ(x) asm volatile("" : "+v"(x))
#define OPQ2(x, y) asm volatile("" : "+v"(x), "+v"(y))
__device__ __forceinline__ float sel(bool c, float a, float b) { asm("" : "+v"(a)); return c ? a : b; }
__device__ __forceinline__ u32 pk2o(float a, float b) { asm("" : "+v"(a)); asm("" : "+v"(b)); return pk2(a, b); }
struct TfPre { u32 aT, aR, oo, bp, bq; };
__device__ __forceinline__ TfPre tfpre(const u32 j0, const u32 slot, const u32 lane) {
  const u32 g = lane >> 4, n = lane & 15, k = n >> 1, nd = n & 1, u = j0 >> 2, kr = 2 * g + ((n >> 2) & 1);
  TfPre p;
  p.aT = chunk(u * 8 + k) + (g ^ ((k >> 2) << 1)) * 64 + ((j0 ^ k) & 3) * 16 + nd * 8;
  p.aR = chunk(u * 8 + kr) + ((n & 3) ^ ((kr >> 2) << 1)) * 64 + ((j0 ^ kr) & 3) * 16 + (n >> 3) * 8;
  p.oo = (slot * 40 + 2 * j0 + nd) * 256 + k * 32 + 8 * g;
  p.bp = (20 * g + nd) * 4; p.bq = (16 * (n >> 2) + n) * 4;
  OPQ2(p.aT, p.aR); OPQ2(p.bp, p.bq); OPQ(p.oo);
  return p;
}
template <int NT, int PQ>
__device__ __forceinline__ void tf(const TfPre& P, const u32 lane, const float dv0, const float dv1, u16* __restrict__ out) {
  const u32 g = lane >> 4, n = lane & 15, k = n >> 1, nd = n & 1;
  const u32 aT = P.aT, bp = P.bp, bq = P.bq, oo = P.oo;
  u32 aW = lane * 16;
  float finf = __builtin_inff(); asm("" : "+s"(finf));
  v4u TF[NT][4], W[RD];
#pragma unroll
  for (int s = 0; s < 4; s++) {
#pragma unroll
    for (int t = 0; t < NT; t++) {
      const v2u a = ld64((aT ^ (16 * t)) + 512 * s), b = ld64((aT ^ (16 * t)) + 512 * s + 256);
      TF[t][s] = v4u{a[0], a[1], b[0], b[1]};
    }
    if (s < RD) W[s] = ld128(aW + L_WR + s * 1024);
  }
  SB();
  u32 aR[2] = {P.aR, P.aR ^ 16};
  OPQ(aR[1]);
  const float dve[2] = {dv0, dv1};
  float rho[NT], rb[NT][2], c1[NT][2], c3[NT][2], v0[NT], v1[NT], mx[2], zs[2], l1[2], l3[2], q2[2][2];
  v4f S[NT], LN[NT], GR[NT], U[NT][8], y[NT][8], o[NT][8];
  v4u ub[NT], B[NT]; v2u Y[NT][8], tr[NT][8]; u32 dg0[NT], dg1[NT];
  const v4f Z4 = {0, 0, 0, 0};
  SB();
  sfor<-4, 68>([&](auto Fc) __attribute__((always_inline)) {
    constexpr int f = decltype(Fc)::value, m = (f >> 2) & 7, s = f & 3, fn = f + RD;
    if constexpr (f == -4) __builtin_amdgcn_s_setprio(PQ & 3);
    if constexpr (f == PS1) __builtin_amdgcn_s_setprio((PQ >> 4) & 3);
    if constexpr (f == PS2) __builtin_amdgcn_s_setprio((PQ >> 8) & 3);
    if constexpr (f >= 10 && f <= 34 && (f & 7) == 2) {
#pragma unroll
      for (int t = 0; t < NT; t++) S[t] = MFMA(TF[t][(f - 10) >> 3], ub[t], (f > 10 ? S[t] : Z4));
      SB();
    }
#pragma unroll
    for (int mm = 0; mm < 8; mm++) {
      if (f == FO(mm)) {
#pragma unroll
        for (int t = 0; t < NT; t++) o[t][mm] = MFMA((v4u{tr[t][mm][0], tr[t][mm][1], Y[t][mm][0], Y[t][mm][1]}), B[t], Z4);
        SB();
      }
    }
    if constexpr (f < 64) {
      const v4u wf = W[(f + 4) % RD];
#pragma unroll
      for (int t = 0; t < NT; t++) {
        if constexpr (f < 0) { GR[t] = MFMA(TF[t][s], TF[t][s], (s ? GR[t] : Z4)); LN[t] = MFMA(wf, TF[t][s], (s ? LN[t] : Z4)); }
        else if constexpr (f < 32) U[t][m] = MFMA(wf, TF[t][s], (s ? U[t][m] : Z4));
        else y[t][m] = MFMA(TF[t][s], wf, (s ? y[t][m] : Z4));
      }
      if constexpr (fn == 32) { aW += L_WW; OPQ(aW); }
      if constexpr (fn < 64)
      W[(f + 4) % RD] = ld128(aW + (fn < 0 ? L_WR + (fn + 4) * 1024 : (fn & 31) * 1024));
    }
#pragma unroll
    for (int t = 0; t < NT; t++) {
      if (f == 1 + 2 * t) {
        const float qa = sel(nd, GR[t][1], GR[t][0]), qb = sel(nd, GR[t][3], GR[t][2]), q = (n & 2) ? qb : qa;
        const float a = LN[t][0] * (1.f / 128.f);
        rho[t] = fmaxf(q * (1.f / 128.f) - a * a, 0.f) + (k == 0 ? 1e-5f : 1e-5f * dve[t] * dve[t]);
        OPQ(rho[t]);
      }
      if (f == 2 + 2 * t) {
        rho[t] = __builtin_amdgcn_rsqf(rho[t]); rho[t] = k == 0 ? rho[t] : __builtin_copysignf(rho[t], dve[t]);
        const float dd = g < 2 ? (k == 0 ? dve[t] : 1.f) : 0.f;
        l1[t] = 0.5f * dd * LN[t][0]; l3[t] = dd * LN[t][1];
        OPQ2(l1[t], l3[t]);
      }
      if (f == 6 + t) {
        rb[t][0] = bperm(bp, rho[t]); rb[t][1] = bperm(bp + 8, rho[t]); rho[t] = bperm(bq, rho[t]);
      }
      if (f == 8 + t) {
        const u32 d1 = k == 0 ? __float_as_uint(dve[t]) : 0x3f800000u, d2 = (k >> 1) == g ? d1 : 0u, dg = d2 >> ((n & 2) ? 0 : 16);
        dg0[t] = nd ? 0u : dg; dg1[t] = nd ? dg : 0u; OPQ2(dg0[t], dg1[t]);
      }
      if (f == 41) { c1[t][0] = bperm(bp, LN[t][2]); c1[t][1] = bperm(bp + 8, LN[t][2]); c3[t][0] = bperm(bp, LN[t][3]); c3[t][1] = bperm(bp + 8, LN[t][3]); }
      if (f == 12) { v0[t] = rb[t][0] * rho[t]; v1[t] = rb[t][1] * rho[t]; OPQ2(v0[t], v1[t]); rb[t][0] *= dve[t]; rb[t][1] *= dve[t]; OPQ2(rb[t][0], rb[t][1]); }
    }
    if constexpr (f >= 5 && f <= 33 && s == 1) {
      constexpr int mm = (f - 5) >> 2;
#pragma unroll
      for (int t = 0; t < NT; t++) { ub[t][(mm & 1) * 2] = pk2(U[t][mm][0], U[t][mm][1]); ub[t][(mm & 1) * 2 + 1] = pk2(U[t][mm][2], U[t][mm][3]); }
    }
    if (f == 35) {
#pragma unroll
      for (int t = 0; t < NT; t++) { v0[t] *= sel(nd, S[t][1], S[t][0]); v1[t] *= sel(nd, S[t][3], S[t][2]); OPQ2(v0[t], v1[t]); mx[t] = fmx(v0[t], v1[t]); }
      if (NT == 1) mx[1] = mx[0];
      sw16(mx[0], mx[1]); mx[0] = fmx(mx[0], mx[1]);
    }
    if (f == 36) { mx[1] = mx[0]; sw32(mx[0], mx[1]); mx[0] = fmx(mx[0], mx[1]); mx[1] = mx[0]; if (NT == 2) sw16(mx[0], mx[1]); }
    if (f == 36) {
#pragma unroll
      for (int t = 0; t < NT; t++) { v0[t] = __builtin_amdgcn_exp2f(v0[t] - mx[t]); v1[t] = __builtin_amdgcn_exp2f(v1[t] - mx[t]); zs[t] = v0[t] + v1[t]; }
      if (NT == 1) zs[1] = zs[0];
    }
    if (f == 37) { sw16(zs[0], zs[1]); zs[0] += zs[1]; zs[1] = zs[0]; sw32(zs[0], zs[1]); zs[0] = __builtin_amdgcn_rcpf(zs[0] + zs[1]); zs[1] = zs[0]; if (NT == 2) sw16(zs[0], zs[1]); }
    if (f == 38) {
#pragma unroll
      for (int t = 0; t < NT; t++) {
        v0[t] *= rb[t][0]; v1[t] *= rb[t][1]; OPQ(v0[t]); v0[t] *= zs[t]; v1[t] *= zs[t];
        const u32 p = pk2o(v0[t], v1[t]);
        B[t] = v4u{dg0[t], dg1[t], nd ? 0u : p, nd ? p : 0u};
      }
    }
    if (f == 44) {
#pragma unroll
      for (int t = 0; t < NT; t++) {
        l1[t] += v0[t] * c1[t][0]; l3[t] += v0[t] * c3[t][0]; OPQ2(l1[t], l3[t]); l1[t] += v1[t] * c1[t][1]; l3[t] += v1[t] * c3[t][1];
        OPQ2(l1[t], l3[t]);
      }
    }
    if (f == 48) {
#pragma unroll
      for (int t = 0; t < NT; t++) { sw32(l1[t], l3[t]); l1[t] += l3[t]; }
      if (NT == 1) l1[1] = l1[0];
      sw16(l1[0], l1[1]); l1[0] += l1[1];
    }
    if (f == 52) { l3[0] = l1[0]; sw32(l1[0], l3[0]); l1[0] *= 1.f / 128.f; OPQ2(l1[0], l3[0]); }
#pragma unroll
    for (int mm = 0; mm < 8; mm++) {
      const int fo = FO(mm);
      if (f == fo - 3) {
#pragma unroll
        for (int t = 0; t < NT; t++) tr[t][mm] = __builtin_bit_cast(v2u, rd(aR[t] + 256 * mm));
      }
      if (f == 37 + 4 * mm - (mm == 7)) {
#pragma unroll
        for (int t = 0; t < NT; t++) Y[t][mm] = v2u{pk2(y[t][mm][0], y[t][mm][2]), pk2(y[t][mm][1], y[t][mm][3])};
      }
#pragma unroll
      for (int h = 0; h < 2; h++) {
        if (f == fo + 1 + h) {
#pragma unroll
          for (int t = 0; t < NT; t++) {
            const float a = o[t][mm][2 * h], c = o[t][mm][2 * h + 1];
            if (mm == 0 && h == 0) { q2[t][0] = a * a; q2[t][1] = c * c; } else { q2[t][0] += a * a; q2[t][1] += c * c; }
            OPQ2(q2[t][0], q2[t][1]);
          }
        }
      }
    }
    SB();
  });
#pragma unroll
  for (int m = 0; m < 8; m++)
#pragma unroll
    for (int t = 0; t < NT; t++) tr[t][m] = __builtin_bit_cast(v2u, rd(aR[t] + 256 * m));
  const float dr = (NT == 2 && (g & 1)) ? dve[1] : dve[0];
  float al;
  {
    float qa = q2[0][0] + q2[0][1], qb = q2[NT - 1][0] + q2[NT - 1][1];
    sw16(qa, qb); qa += qb; qb = qa; sw32(qa, qb);
    const float var = fmaxf((qa + qb) * (1.f / 128.f) - l1[0] * l1[0], 0.f);
    const float lg = l3[0] * __builtin_copysignf(__builtin_amdgcn_rsqf(var + 1e-5f * dr * dr), dr);
    float mm = fmx(lg, DPPF(lg, 0x122)); mm = fmx(mm, DPPF(mm, 0x124)); mm = fmx(mm, DPPF(mm, 0x128));
    const float e = __builtin_amdgcn_exp2f(lg - mm);
    float z = e + DPPF(e, 0x122); z += DPPF(z, 0x124); z += DPPF(z, 0x128);
    al = e * __builtin_amdgcn_rcpf(z);
  }
  const float ald = al * __builtin_amdgcn_rcpf(dr), als = k == 0 ? al : ald;
  float aa[2] = {ald, ald}; if (NT == 2) sw16(aa[0], aa[1]);
  const u32 br = (4 * g + nd) * 4;
  v4f R[NT][2];
#pragma unroll
  for (int t = 0; t < NT; t++) {
    const float a0 = bperm(br + 64 * t, als), a1 = bperm(br + 64 * t + 8, als);
    const u32 p = B[t][2] | B[t][3];
    float b0 = bflo(p) * aa[t], b1 = bfhi(p) * aa[t];
    OPQ(b0);
    b0 += DPPF(b0, 0x122); b1 += DPPF(b1, 0x122); b0 += DPPF(b0, 0x124); b1 += DPPF(b1, 0x124); b0 += DPPF(b0, 0x128); b1 += DPPF(b1, 0x128);
    const u32 xa = pk2o(a0, a1), xb = pk2o(b0, b1);
#pragma unroll
    for (int c = 0; c < 4; c++) {
      const bool e0 = (n & 13) == 4 * c, e1 = (n & 13) == 4 * c + 1;
      const v4u gb = {e0 ? xa : 0u, e1 ? xa : 0u, e0 ? xb : 0u, e1 ? xb : 0u};
#pragma unroll
      for (int j = 0; j < 2; j++) R[t][j] = MFMA((v4u{tr[t][2 * c + j][0], tr[t][2 * c + j][1], Y[t][2 * c + j][0], Y[t][2 * c + j][1]}), gb, (c ? R[t][j] : Z4));
    }
  }
#pragma unroll
  for (int t = 0; t < NT; t++) {
    const u32 r0 = pk2(R[t][0][0], R[t][0][1]), r1 = pk2(R[t][0][2], R[t][0][3]), r2 = pk2(R[t][1][0], R[t][1][1]), r3 = pk2(R[t][1][2], R[t][1][3]);
    __builtin_amdgcn_raw_buffer_store_b64(v2u{(n & 2) ? r2 : r0, (n & 2) ? r3 : r1}, __builtin_amdgcn_make_buffer_rsrc(out, 0, NN * 256, 0x00020000), oo + 512 * t, 0, 0);
  }
}
#define P3A 0x123
#define P3B 0x123
#define P3C 0x123

#define K3T 768
__global__ void __launch_bounds__(K3T) k3(const u16* __restrict__ x, const u16* __restrict__ dis, char* __restrict__ ws, u16* __restrict__ out) {
#define KID 3
  __shared__ __attribute__((aligned(16))) char T[163840];
  const int t = threadIdx.x, lane = t & 63, w = __builtin_amdgcn_readfirstlane(t >> 6), slot = blockIdx.x;
  asm volatile("" :: "v"((u32)(size_t)(LAS char*)T));
  STAMP_INIT STAMP(0);
  const v4u srd = {(u32)(size_t)ws, (u32)((size_t)ws >> 32) & 0xffffu, WS_DBG, 0x00020000u};
  v4u xv = {0, 0, 0, 0};
  if (t < 640) xv = *(const v4u*)(x + (size_t)slot * 5120 + t * 8);
  const u32 j0 = w < 8 ? 2 * w : min(w + 8, 19), two = w < 8 ? 2 : 0;
  const u16 dq0 = dis[slot * 40 + 2 * j0 + (lane & 1)], dq1 = dis[slot * 40 + 2 * j0 + two + (lane & 1)];
  for (int id = w; id < 70; id += K3T / 64) {
    const u32 k = id / 10 + 1, rem = id - (k - 1) * 10, u = rem >> 1, h = rem & 1;
    const u32 vo = ((lane >> 2) ^ ((k >> 2) << 1)) * GSTRIDE + ((lane ^ k) & 3) * 16;
    const u32 so = WS_G + k * GBUF + h * 16 * GSTRIDE + (u < 4 ? (u >> 1) * 32768 + 128 * slot + (u & 1) * 64 : 65536 + 64 * slot);
    DMA0(vo, so, chunk(u * 8 + k) + h * 1024);
  }
  for (int id = w; id < 68; id += K3T / 64) DMA0(lane * 16, WS_W + id * 1024, id < 32 ? id * 1024 : (id < 64 ? L_WW + (id - 32) * 1024 : L_WR + (id - 64) * 1024));
  if (t < 640) {
    const u32 node = t >> 4, a = chunk((node >> 3) * 8) + (t & 15) * 128 + ((node >> 1) & 3) * 16 + (node & 1) * 8;
    *(LAS v2u*)(size_t)a = v2u{xv[0], xv[1]}; *(LAS v2u*)(size_t)(a + 64) = v2u{xv[2], xv[3]};
  }
  const TfPre P = tfpre(j0, slot, lane);
  float dv0 = bf2f(dq0), dv1 = bf2f(dq1);
  dv0 = dv0 == 0.f ? 1.f : dv0; dv1 = dv1 == 0.f ? 1.f : dv1; OPQ2(dv0, dv1);
  asm volatile("" ::: "memory");
  __builtin_amdgcn_s_waitcnt(0x0070);
  __builtin_amdgcn_s_barrier();
  asm volatile("" ::: "memory");
  STAMP(1);
  if (w < 4) tf<2, P3A>(P, lane, dv0, dv1, out);
  else if (w < 8) tf<2, P3B>(P, lane, dv0, dv1, out);
  else if (w < 12) tf<1, P3C>(P, lane, dv0, dv1, out);
  STAMP(2);
  STAMP_END;
#undef KID
}
__device__ __host__ __forceinline__ u32 rowof(u32 n) { u32 s = n / 40, l = n - s * 40; return l < 32 ? (l >> 4) * 4096 + 16 * s + (l & 15) : 8192 - 32 + 8 * s + l; }

extern "C" void kernel_launch(void* const* d_in, const int* in_sizes, int n_in, void* d_out, int out_size, void* d_ws, size_t ws_size, hipStream_t stream) {
  const u16* x = (const u16*)d_in[0]; const int* src = (const int*)d_in[1]; const int* dst = (const int*)d_in[2]; const u16* dis = (const u16*)d_in[3];
  const u16* wq = (const u16*)d_in[4]; const u16* wk = (const u16*)d_in[5]; const u16* wv = (const u16*)d_in[6]; const u16* wo = (const u16*)d_in[7]; const u16* wr = (const u16*)d_in[8];
  char* ws = (char*)d_ws;
  k1<<<253, 640, 0, stream>>>(src, dst, x, dis, wq, wk, wv, wo, ws);
  k2<<<256, 1024, 0, stream>>>(dis, wq, wk, wv, wo, wr, ws);
  k3<<<250, K3T, 0, stream>>>(x, dis, ws, (u16*)d_out);
}
#define WS_TOK WS_G
#define TOKBYTES (8u * GBUF)
#define TOKSC(n) hbf2f(dis[n])
#define TOKADDR(n,k,d) ((size_t)(k) * GBUF + ((d) >> 2) * GSTRIDE + rowof(n) * 8 + ((d) & 3) * 2)
```

```cpp
#include <hip/hip_runtime.h>
#include <hip/hip_bf16.h>
#include <stdint.h>
#include <type_traits>

#define OLDBAR
#define NN 10000
#define EE 320000
typedef unsigned short u16;
typedef unsigned u32;
typedef unsigned long long u64;
typedef u32 v4u __attribute__((ext_vector_type(4)));
typedef u32 v2u __attribute__((ext_vector_type(2)));
typedef short v4s __attribute__((ext_vector_type(4)));
typedef float v4f __attribute__((ext_vector_type(4)));
typedef __bf16 v4bf __attribute__((ext_vector_type(4)));
typedef __bf16 v8bf __attribute__((ext_vector_type(8)));
typedef __bf16 v16bf __attribute__((ext_vector_type(16)));
typedef __bf16 v2bf __attribute__((ext_vector_type(2)));
typedef float v2f __attribute__((ext_vector_type(2)));
typedef u16 v2h __attribute__((ext_vector_type(2)));
#define LAS __attribute__((address_space(3)))

#define GSTRIDE 81920u
#define GBUF    (32u * GSTRIDE)
#define WS_G    0u
#define WS_SUB  (8u * GBUF)
#define WS_ELL  (WS_SUB + 4096000u)
#define WS_HDR  (WS_ELL + 1280000u)
#define WS_CNT  (WS_HDR + 2048u)
#define WS_OVF1 (WS_CNT + 256u)
#define WS_OVF2 (WS_OVF1 + 128u * 2504u * 4u)
#define WS_W    (WS_OVF2 + 1280000u)
#define WS_DBG  (WS_W + 69632u)
#define POISON 0xFFFFFFFFu
#define OOB 0x40000000u
#define ZROW 15312u
#define MAXSPIN 100000u
#define AUX1 16

#define STAMP_INIT
#define STAMP(i)
#define STAMP_END

#define WST
#define WSA
#define PSTAMP(i) STAMP(i)
__device__ __forceinline__ float bf2f(u16 v) { return __uint_as_float(((u32)v) << 16); }
__device__ __forceinline__ float bflo(u32 v) { return __uint_as_float(v << 16); }
__device__ __forceinline__ float bfhi(u32 v) { return __uint_as_float(v & 0xffff0000u); }
__device__ __forceinline__ u32 pk2(float a, float b) { v2f f = {a, b}; v2bf r = __builtin_convertvector(f, v2bf); return __builtin_bit_cast(u32, r); }
__device__ __forceinline__ u32 pk2s(float a, float b) { return min(pk2(a, b), 0xFFFFFFFEu); }
#define DPPU(v, ctrl) ((u32)__builtin_amdgcn_mov_dpp((int)(v), (ctrl), 0xF, 0xF, true))
#define LBAR() do { asm volatile("" ::: "memory"); __builtin_amdgcn_s_waitcnt(0xC07F); __builtin_amdgcn_s_barrier(); asm volatile("" ::: "memory"); } while (0)
__device__ __forceinline__ u32 tabidx(u32 n) {
  u32 s = (n * 52429u) >> 21, l = n - s * 40u;
  u32 row = l < 32u ? ((l >> 4) << 12) + 16u * s + (l & 15u) : 8192u - 32u + 8u * s + l;
  return row + (row >= 5120u ? 5120u : 0u);
}

#define DMA0(voff, soff, lbase) asm volatile("s_mov_b32 m0, %2\n\ts_nop 0\n\tbuffer_load_dwordx4 %0, %3, %1 offen lds" :: "v"(voff), "s"(soff), "s"(lbase), "s"(srd) : "memory")
__device__ __forceinline__ v4bf rd(u32 a) { return __builtin_amdgcn_ds_read_tr16_b64_v4bf16((LAS v4bf*)(size_t)a); }
#define MFMA(a, b, c) __builtin_amdgcn_mfma_f32_16x16x32_bf16(__builtin_bit_cast(v8bf, (v4u)(a)), __builtin_bit_cast(v8bf, (v4u)(b)), (c), 0, 0, 0)
__device__ __forceinline__ void sw16(float& a, float& b) { const v2u r = __builtin_amdgcn_permlane16_swap(__float_as_uint(a), __float_as_uint(b), false, false); a = __uint_as_float(r[0]); b = __uint_as_float(r[1]); }
__device__ __forceinline__ void sw32(float& a, float& b) { const v2u r = __builtin_amdgcn_permlane32_swap(__float_as_uint(a), __float_as_uint(b), false, false); a = __uint_as_float(r[0]); b = __uint_as_float(r[1]); }
#define DPPF(v, ctrl) __uint_as_float(DPPU(__float_as_uint(v), ctrl))
#define LOG2E 1.4426950408889634f
template <class F>
__device__ __forceinline__ void wprep(const int which, const u16* wq, const u16* wk, const u16* wv, const u16* wo, const u16* wr, char* ws, float* L, F&& early WST) {
  const int t = threadIdx.x, lane = t & 63, m = __builtin_amdgcn_readfirstlane(t >> 6), g = lane >> 4, i = lane & 15;
  v4f D[8];
  v4u B[4];
  const float wra = bf2f(wr[lane]), wrb = bf2f(wr[64 + lane]), wrm = bf2f(wr[16 * (m & 7) + i]);
  const u32 lb = (u32)(size_t)(LAS float*)L;
  if (m < 8) {
    const u16* wa = which == 0 ? wq : wv;
    {
      const v4u srd = {(u32)(size_t)wa, (u32)((size_t)wa >> 32) & 0xffffu, 32768u, 0x00020000u};
#pragma unroll
      for (int ks = 0; ks < 4; ks++) DMA0(i * 256 + g * 16, m * 4096 + ks * 64, lb + (m * 4 + ks) * 1024);
    }
    if (which == 0) {
#pragma unroll
      for (int ks = 0; ks < 4; ks++) B[ks] = *(const v4u*)(wk + (16 * m + i) * 128 + 32 * ks + 8 * g);
    } else {
      const v4u srd = {(u32)(size_t)wo, (u32)((size_t)wo >> 32) & 0xffffu, 32768u, 0x00020000u};
#pragma unroll
      for (int ks = 0; ks < 4; ks++) DMA0(lane * 16, (m * 4 + ks) * 1024, lb + 81920 + (m * 4 + ks) * 1024);
    }
  }
  asm volatile("" ::: "memory");
  __builtin_amdgcn_s_waitcnt(0x0070);
  __builtin_amdgcn_s_barrier();
  asm volatile("" ::: "memory");
  early();
  STAMP(40);
  if (m < 8) {
    if (which == 1) {
#pragma unroll
      for (int ks = 0; ks < 4; ks++) {
        const u32 a = lb + 81920 + (32 * ks + 8 * g + (i >> 2)) * 256 + m * 32 + (i & 3) * 8;
        const v2u lo = __builtin_bit_cast(v2u, rd(a)), hi = __builtin_bit_cast(v2u, rd(a + 1024));
        B[ks] = v4u{lo[0], lo[1], hi[0], hi[1]};
      }
    }
    float cs = 0.f;
#pragma unroll
    for (int dt = 0; dt < 8; dt++) {
      v4f a = {0, 0, 0, 0};
#pragma unroll
      for (int ks = 0; ks < 4; ks++) a = MFMA(*(const LAS v4u*)(size_t)(lb + (dt * 4 + ks) * 1024 + lane * 16), B[ks], a);
      D[dt] = a; cs += (a[0] + a[1]) + (a[2] + a[3]);
    }
    STAMP(41);
    float c2 = cs; sw16(cs, c2); cs += c2; c2 = cs; sw32(cs, c2); cs = (cs + c2) * (1.f / 128.f);
#pragma unroll
    for (int dt = 0; dt < 8; dt++) D[dt] -= cs;
    float wc = 0.f;
    if (which == 1) {
      float z = wra + wrb; z += DPPF(z, 0x121); z += DPPF(z, 0x122); z += DPPF(z, 0x124); z += DPPF(z, 0x128);
      c2 = z; sw16(z, c2); z += c2; c2 = z; sw32(z, c2); z = (z + c2) * (1.f / 128.f);
      wc = (wrm - z) * LOG2E;
      if (g == 0) L[28672 + 16 * m + i] = wc;
    }
#pragma unroll
    for (int pass = 0; pass < 2; pass++) {
      if (pass == 0 || which == 1) {
        v4f r[8];
#pragma unroll
        for (int dt = 0; dt < 8; dt++) r[dt] = pass ? D[dt] * wc : D[dt];
        __builtin_amdgcn_sched_barrier(0);
#pragma unroll
        for (int st = 0; st < 4; st++) {
#pragma unroll
          for (int dt = 0; dt < 8; dt++)
#pragma unroll
            for (int q = 0; q < 4; q++) { const float z = r[dt][q]; r[dt][q] = z + (st == 0 ? DPPF(z, 0x121) : st == 1 ? DPPF(z, 0x122) : st == 2 ? DPPF(z, 0x124) : DPPF(z, 0x128)); }
          __builtin_amdgcn_sched_barrier(0);
        }
        if (i == 0) {
#pragma unroll
          for (int dt = 0; dt < 8; dt++) *(v4f*)(L + 8192 + pass * 1024 + m * 128 + dt * 16 + 4 * g) = r[dt];
        }
      }
    }
  }
  STAMP(42);
  LBAR();
  if (t < 256) {
    float z = 0.f;
#pragma unroll
    for (int q = 0; q < 8; q++) z += L[8192 + (t >> 7) * 1024 + q * 128 + (t & 127)];
    L[t] = z;
  }
  asm volatile("s_waitcnt vmcnt(0)" ::: "memory");
  LBAR();
  if (which == 0) {
    if (m < 8) {
#pragma unroll
      for (int dt = 0; dt < 8; dt++) D[dt] = (D[dt] - *(const v4f*)(L + dt * 16 + 4 * g) * (1.f / 128.f)) * (LOG2E * 0.08838834764831845f);
    }
  } else if (m < 4) {
    const float* p = L + ((i & 3) == 1 ? 28672 : ((i & 1) ? 128 : 0)) + 32 * m + 4 * g;
    v4f a = *(const v4f*)p, c = *(const v4f*)(p + 16);
    v4u o = {pk2(a[0], a[1]), pk2(a[2], a[3]), pk2(c[0], c[1]), pk2(c[2], c[3])};
    if ((i & 7) == 5) o = v4u{pk2(a[0] - bflo(o[0]), a[1] - bfhi(o[0])), pk2(a[2] - bflo(o[1]), a[3] - bfhi(o[1])), pk2(c[0] - bflo(o[2]), c[1] - bfhi(o[2])), pk2(c[2] - bflo(o[3]), c[3] - bfhi(o[3]))};
    if ((i & 3) == 0) o = v4u{0x3f803f80u, 0x3f803f80u, 0x3f803f80u, 0x3f803f80u};
    *(v4u*)(ws + WS_W + 65536 + m * 1024 + lane * 16) = o;
  }
  STAMP(43);
  if (m < 8) {
#pragma unroll
    for (int s = 0; s < 4; s++) {
      const v4u o = {pk2(D[2 * s][0], D[2 * s][1]), pk2(D[2 * s][2], D[2 * s][3]), pk2(D[2 * s + 1][0], D[2 * s + 1][1]), pk2(D[2 * s + 1][2], D[2 * s + 1][3])};
      *(v4u*)(ws + WS_W + which * 32768 + (m * 4 + s) * 1024 + lane * 16) = o;
    }
  }
}

__global__ void __launch_bounds__(640) k1(const int* __restrict__ src, const int* __restrict__ dst, const u16* __restrict__ x,
                                          const u16* __restrict__ dis, const u16* __restrict__ wq, const u16* __restrict__ wk,
                                          const u16* __restrict__ wv, const u16* __restrict__ wo, char* __restrict__ ws) {
#define KID 0
  __shared__ __attribute__((aligned(16))) u32 L[8704];
  const int t = threadIdx.x, b = blockIdx.x;
  const __amdgpu_buffer_rsrc_t rs = __builtin_amdgcn_make_buffer_rsrc(ws, 0, WS_DBG, 0x00020000);
  STAMP_INIT STAMP(0);
  if (b < 128) {
    u32* stage = L; u32* cnt = L + 8000; u32* novf = L + 8256;
    v4u s4 = {0, 0, 0, 0}, d4 = {0, 0, 0, 0};
    if (t < 625) { s4 = *(const v4u*)(src + b * 2500 + t * 4); d4 = *(const v4u*)(dst + b * 2500 + t * 4); }
    u32 pf = 0;
    if (t >= 632) pf = *(const u32*)(((b & 64) ? ((b & 32) ? wo : wv) : ((b & 32) ? wk : wq)) + (b & 31) * 512 + (t - 632) * 64);
    if (t < 257) cnt[t] = 0;
    if (b == 0 && t == 0) *(u32*)(ws + WS_CNT) = 0;
    __syncthreads();
    STAMP(1);
    u32* ovf = (u32*)(ws + WS_OVF1) + b * 2504;
    if (t < 625) {
      u32 r[4], bin[4], en[4];
#pragma unroll
      for (int i = 0; i < 4; i++) {
        u32 d = d4[i];
        bin[i] = (d * 52429u) >> 21;
        r[i] = atomicAdd(&cnt[bin[i]], 1u);
        en[i] = tabidx(s4[i]) | ((d - bin[i] * 40u) << 16);
      }
#pragma unroll
      for (int i = 0; i < 4; i++) {
        if (r[i] < 31) stage[bin[i] * 32 + 1 + r[i]] = en[i];
        else { u32 o = atomicAdd(novf, 1u); ovf[4 + o] = (en[i] & 0xffffu) | (d4[i] << 16); }
      }
    }
    __syncthreads();
    STAMP(2);
    if (t == 0) ovf[0] = *novf;
    asm volatile("" :: "v"(pf));
#pragma unroll
    for (int it = 0; it < 4; it++) {
      int i = t + it * 640;
      if (i < 2000) {
        int bin = i >> 3, q = i & 7;
        u32 c = cnt[bin];
        if ((u32)(q * 4) <= c) {
          v4u v = *(const v4u*)(stage + bin * 32 + q * 4);
          if (q == 0) v[0] = c;
          __builtin_amdgcn_raw_buffer_store_b128(v, rs, WS_SUB + b * 128 + bin * 16384 + q * 16, 0, AUX1);
        }
      }
    }
    STAMP(3);
  } else {
    const int wb = b - 128;
    char* S = (char*)L;
    v4u v[2]; float dv[2]; u32 go[2];
#pragma unroll
    for (int it = 0; it < 2; it++) {
      int i = t + it * 640; int node = i >> 4, cq = i & 15;
      v[it] = *(const v4u*)(x + (size_t)(wb * 80 + node) * 128 + cq * 8);
      dv[it] = bf2f(dis[wb * 80 + node]);
    }
#pragma unroll
    for (int it = 0; it < 2; it++) {
      int i = t + it * 640; int g = i / 40, pc = i - g * 40;
      go[it] = g * GSTRIDE + (pc < 32 ? (pc >> 4) * 32768 + wb * 256 + (pc & 15) * 16 : 65536 + wb * 128 + (pc - 32) * 16);
    }
#pragma unroll
    for (int it = 0; it < 2; it++) {
      int i = t + it * 640; int node = i >> 4, cq = i & 15;
      int s = node >= 40, l = node - s * 40;
      int pos = l < 32 ? (l >> 4) * 32 + s * 16 + (l & 15) : 32 + s * 8 + l;
      v2u a, c;
      a[0] = pk2s(bflo(v[it][0]) * dv[it], bfhi(v[it][0]) * dv[it]);
      a[1] = pk2s(bflo(v[it][1]) * dv[it], bfhi(v[it][1]) * dv[it]);
      c[0] = pk2s(bflo(v[it][2]) * dv[it], bfhi(v[it][2]) * dv[it]);
      c[1] = pk2s(bflo(v[it][3]) * dv[it], bfhi(v[it][3]) * dv[it]);
      *(v2u*)(S + (2 * cq) * 656 + pos * 8) = a;
      *(v2u*)(S + (2 * cq + 1) * 656 + pos * 8) = c;
    }
    __syncthreads();
    STAMP(2);
#pragma unroll
    for (int it = 0; it < 2; it++) {
      int i = t + it * 640; int g = i / 40, pc = i - g * 40;
      v4u o = *(const v4u*)(S + g * 656 + pc * 16);
      __builtin_amdgcn_raw_buffer_store_b128(o, rs, WS_G + go[it], 0, AUX1);
    }
    const v4u pz = {POISON, POISON, POISON, POISON};
#pragma unroll
    for (int it = 0; it < 4; it++) __builtin_amdgcn_raw_buffer_store_b128(pz, rs, WS_G + go[it & 1], (1 + (it >> 1)) * GBUF, 16);
    __builtin_amdgcn_raw_buffer_store_b128(pz, rs, WS_ELL + wb * 10240 + t * 16, 0, 16);
    if (t == 0) __builtin_amdgcn_raw_buffer_store_b128(pz, rs, WS_HDR + wb * 16, 0, 16);
    STAMP(3);
  }
  STAMP_END;
#undef KID
}

#define B_ELL 0
#define B_CB 1280
#define B_CN 1472
#define B_BUSY 2240
#define B_FULL 2432
#define B_ROWM 2480
#define B_NT 2576
#define B_NF 2577
#define B_Z0 1280
#define B_ZN 1304
#define B_Q 2592
#define B_F 20480
#define FID t
#define B_N1 2578
#define B_NM 2579
#define B_NO 2580
#define B_M2 2581
#define B_BASE 2582
template <class F>
__device__ __forceinline__ void build(const int slot, char* __restrict__ ws, u32* L, F&& early WST) {
  const int t = threadIdx.x, lane = t & 63, w = __builtin_amdgcn_readfirstlane(t >> 6);
  const __amdgpu_buffer_rsrc_t rs = __builtin_amdgcn_make_buffer_rsrc(ws, 0, WS_DBG, 0x00020000);
  v4u v; u32 n1;
  {
    const v4u srd = {(u32)(size_t)ws, (u32)((size_t)ws >> 32) & 0xffffu, WS_DBG, 0x00020000u};
    asm volatile("buffer_load_dwordx4 %0, %2, %4, %5 offen\n\tbuffer_load_dword %1, %3, %4, 0 offen"
                 : "=&v"(v), "=&v"(n1) : "v"(t * 16), "v"(t < 128 ? WS_OVF1 + t * 10016u : OOB), "s"(srd), "s"(WS_SUB + slot * 16384) : "memory");
  }
  if (t < 320) { const v4u z = {POISON, POISON, POISON, POISON}; *(v4u*)(L + B_ELL + t * 4) = z; }
  else if (t < 320 + B_ZN / 4) { const v4u z = {0, 0, 0, 0}; *(v4u*)(L + B_Z0 + (t - 320) * 4) = z; }
  LBAR();
  early();
  asm volatile("s_waitcnt vmcnt(5)" : "+v"(v), "+v"(n1) :: "memory");
  STAMP(40);
  {
    const u32 q = t & 7;
    u32 c = DPPU(v[0], 0x00);
    c = (u32)__builtin_amdgcn_update_dpp((int)c, (int)c, 0x114, 0xF, 0xA, false);
    c = min(c, 31u);
    u32 inc = c + (u32)__builtin_amdgcn_update_dpp(0, (int)c, 0x118, 0xF, 0xC, false);
    inc += (u32)__builtin_amdgcn_update_dpp(0, (int)inc, 0x142, 0xA, 0xF, false);
    inc += (u32)__builtin_amdgcn_update_dpp(0, (int)inc, 0x143, 0xC, 0xF, false);
    u32 base = 0;
    if (lane == 63) base = atomicAdd(&L[B_NT], inc);
    base = __builtin_amdgcn_readlane(base, 63);
    const u32 pos = B_Q + base + inc - c + 4 * q - 1;
#pragma unroll
    for (int i = 0; i < 4; i++) {
      const u32 id = 4 * q + i;
      L[(id - 1u < c) ? pos + i : B_Q + 4096 + lane] = v[i];
    }
  }
  if (n1) atomicAdd(&L[B_N1], n1);
  LBAR();
  STAMP(41);
  auto place = [&](u32 e, u32 r, u32 c) {
    const u32 loc = e >> 16, hp = loc >> 3;
    const u32 j = hp == 4 ? 24 + (r >> 1) : (r < 12 ? (hp >> 1) * 12 + r : 20 + (hp >> 1) * 4 + r);
    const u32 half = (hp == 4 ? r : hp) & 1;
    const u32 ln = half * 32 + (loc & 4) * 4 + c * 4 + (loc & 3);
    ((u16*)(L + B_ELL))[(j >> 3) * 512 + ln * 8 + (j & 7)] = (u16)e;
    atomicOr(&L[B_ROWM + half * 40 + j], 1u << ((e + 16) & 31));
  };
  const u32 tot = L[B_NT];
  for (u32 b0 = w * 128; b0 < tot; b0 += 2048) {
    u32 e[2], hb[2], rk[2], r[2], c[2];
#pragma unroll
    for (int i = 0; i < 2; i++) { const u32 id = b0 + i * 64 + lane; e[i] = L[B_Q + id]; if (id >= tot) e[i] = ((40 + (lane & 7)) << 16) | (lane & 31); }
#pragma unroll
    for (int i = 0; i < 2; i++) { hb[i] = (e[i] >> 19) * 32 + (e[i] & 31); rk[i] = atomicAdd(&L[B_CB + hb[i]], 1u); }
#pragma unroll
    for (int i = 0; i < 2; i++) {
      r[i] = e[i] >= (32u << 16) ? (rk[i] + (e[i] >> 1)) & 15 : (rk[i] < 12 ? rk[i] : rk[i] - 12);
      c[i] = atomicAdd(&L[rk[i] < 24 ? B_CN + (e[i] >> 16) * 16 + r[i] : B_CN + 640 + lane], 1u);
    }
#pragma unroll
    for (int i = 0; i < 2; i++) {
      if (e[i] < (40u << 16)) {
        if (rk[i] < 24 && c[i] < 4) {
          place(e[i], r[i], c[i]);
          atomicOr(&L[B_BUSY + hb[i]], 1u << r[i]);
          if (c[i] == 3) atomicOr(&L[B_FULL + (e[i] >> 16)], 1u << r[i]);
        } else L[B_F + atomicAdd(&L[B_NF], 1u)] = e[i];
      }
    }
  }
  LBAR();
  STAMP(42);
  const u32 nf = L[B_NF];
  for (u32 id = FID; id < nf; id += 1024) {
    const u32 e = L[B_F + id], loc = e >> 16, hb = (e >> 19) * 32 + (e & 31);
    u32 rho = (id * 5 + (id >> 2)) & 15, tried = 0;
    const u32 pm = loc >= 32 ? 0xFFFFu : 0xFFFu;
    for (int round = 0;; round++) {
      const u32 bs = __hip_atomic_load(&L[B_BUSY + hb], __ATOMIC_RELAXED, __HIP_MEMORY_SCOPE_WORKGROUP);
      const u32 nfl = ~(__hip_atomic_load(&L[B_FULL + loc], __ATOMIC_RELAXED, __HIP_MEMORY_SCOPE_WORKGROUP) | tried);
      u32 cand = nfl & ~bs & pm;
      const bool strict = cand != 0 && round < 2;
      if (!cand) cand = nfl & pm;
      if (!cand) cand = nfl & ~bs & 0xF000;
      if (!cand) cand = nfl & 0xF000;
      if (!cand) { L[B_Q + atomicAdd(&L[B_NO], 1u)] = (e & 0xffffu) | ((slot * 40 + loc) << 16); break; }
      const u32 r = (rho + __builtin_ctz((cand | (cand << 16)) >> rho)) & 15;
      const u32 ob = atomicOr(&L[B_BUSY + hb], 1u << r);
      const u32 c = atomicAdd(&L[B_CN + loc * 16 + r], 1u);
      if (c == 3) atomicOr(&L[B_FULL + loc], 1u << r);
      if (c < 4 && !(strict && ((ob >> r) & 1))) { place(e, r, c); break; }
      if (c >= 3) tried |= 1u << r;
      rho = (rho + 7) & 15;
    }
  }
  auto scan = [&](auto&& fn) {
    for (int j = 0; j < 128; j++) {
      const u32* o1 = (const u32*)(ws + WS_OVF1) + j * 2504;
      const u32 n = o1[0];
      for (u32 i = t; i < n; i += 1024) { const u32 en = o1[4 + i]; if ((en >> 16) - slot * 40u < 40u) fn(en); }
    }
  };
  const bool sp = L[B_N1] != 0;
  if (sp) scan([&](u32) { atomicAdd(&L[B_NM], 1u); });
  asm volatile("s_waitcnt vmcnt(0)" ::: "memory");
  LBAR();
  STAMP(43);
  const u32 no = L[B_NO], nt = no + L[B_NM];
  u32 base = 0;
  if (nt) {
    if (t == 0) L[B_BASE] = atomicAdd((u32*)(ws + WS_CNT), nt);
    LBAR();
    base = L[B_BASE];
    for (u32 i = t; i < no; i += 1024) __builtin_amdgcn_raw_buffer_store_b32(L[B_Q + i], rs, WS_OVF2 + (base + i) * 4, 0, 16);
    if (sp) scan([&](u32 en) { __builtin_amdgcn_raw_buffer_store_b32(en, rs, WS_OVF2 + (base + no + atomicAdd(&L[B_M2], 1u)) * 4, 0, 16); });
    asm volatile("s_waitcnt vmcnt(0)" ::: "memory");
    LBAR();
  }
  if (t < 320) {
    v4u o = *(const v4u*)(L + B_ELL + t * 4);
    const u32* rm = L + B_ROWM + (lane >> 5) * 40 + w * 8;
    const v4u m0 = *(const v4u*)rm, m1 = *(const v4u*)(rm + 4);
#pragma unroll
    for (int i = 0; i < 4; i++) {
      const u32 a = i < 2 ? m0[2 * i] : m1[2 * i - 4], bb = i < 2 ? m0[2 * i + 1] : m1[2 * i - 3];
      u32 fa, fb;
      asm("v_ffbl_b32 %0, %1" : "=v"(fa) : "v"(~a)); asm("v_ffbl_b32 %0, %1" : "=v"(fb) : "v"(~bb));
      v2h d = {(u16)(fa + ZROW), (u16)(fb + ZROW)};
      const u32 oi = o[i];
      o[i] = __builtin_bit_cast(u32, __builtin_elementwise_min(__builtin_bit_cast(v2h, oi), d));
    }
    __builtin_amdgcn_raw_buffer_store_b128(o, rs, WS_ELL + slot * 5120 + t * 16, 0, 16);
  }
  STAMP(5);
  if (t == 320) __builtin_amdgcn_raw_buffer_store_b64(v2u{base, nt}, rs, WS_HDR + slot * 8, 0, 16);
}

__device__ __forceinline__ v16bf rd4(u32 a0, u32 a1, u32 a2, u32 a3) {
  const v4bf a = rd(a0), b = rd(a1), c = rd(a2), d = rd(a3);
  return __builtin_shufflevector(__builtin_shufflevector(a, b, 0, 1, 2, 3, 4, 5, 6, 7), __builtin_shufflevector(c, d, 0, 1, 2, 3, 4, 5, 6, 7),
                                 0, 1, 2, 3, 4, 5, 6, 7, 8, 9, 10, 11, 12, 13, 14, 15);
}
#define DMA(voff, soff, lbase) asm volatile("s_mov_b32 m0, %2\n\ts_nop 0\n\tbuffer_load_dwordx4 %0, %3, %1 offen sc1 lds" :: "v"(voff), "s"(soff), "s"(__builtin_amdgcn_readfirstlane(lbase)), "s"(srd) : "memory")
#define PFM 1
#define PR1 0
#define PR2 2
#define PR3 4
#define ELLAUX 16
#define NPRE 1
#define KMAX 7
#define LASTP 6
#define D23POS 0
#define D4POS 2
#define T2PRE
__global__ void __launch_bounds__(1024) k2(const u16* __restrict__ dis, const u16* __restrict__ wq, const u16* __restrict__ wk, const u16* __restrict__ wv,
                                           const u16* __restrict__ wo, const u16* __restrict__ wr, char* __restrict__ ws) {
#define KID 2
  __shared__ __attribute__((aligned(16))) char T[163840];
  const int t = threadIdx.x, lane = t & 63, w = __builtin_amdgcn_readfirstlane(t >> 6);
  const int b = blockIdx.x, gp = (b & 7) * 2 + (b >> 7), p = (b >> 3) & 15;
  const int slot = p * 16 + w;
  const bool act = slot < 250;
  asm volatile("" :: "v"((u32)(size_t)(LAS char*)T));
  STAMP_INIT STAMP(0);
  const __amdgpu_buffer_rsrc_t rs = __builtin_amdgcn_make_buffer_rsrc(ws + WS_G, 0, 8u * GBUF, 0x00020000);
  const v4u srd = {(u32)(size_t)(ws + WS_G), (u32)((size_t)(ws + WS_G) >> 32) & 0xffffu, 8u * GBUF, 0x00020000u};
  const u32 sgA = gp * 2 * GSTRIDE;
  const u32 vla = w * 1024 + lane * 16;
  const u32 vlc = (w == 15 && lane >= 40) ? OOB : vla;
  const u32 lb0 = w * 1024, lb2 = w * 1024 + 32768 + (w >= 8 ? 40960 : 0);
  auto early = [&]() __attribute__((always_inline)) {
    DMA(vla, sgA, lb0); DMA(vla, sgA + 16384, lb0 + 16384); DMA(vla, sgA + 32768, lb2); DMA(vla, sgA + 49152, lb0 + 90112); DMA(vlc, sgA + 65536, lb0 + 106496);
  };
  {
    const int bs = p * 16 + gp;
    if (bs < 250) build(bs, ws, (u32*)(T + 40960), early WSA);
    else {
      if (bs < 252) wprep(bs - 250, wq, wk, wv, wo, wr, ws, (float*)(T + 40960), early WSA);
      else { early(); asm volatile("s_waitcnt vmcnt(0)" ::: "memory"); }
    }
  }
  if (t < 32) { const v4u z = {0, 0, 0, 0}; *(LAS v4u*)(size_t)(ZROW * 8 + (t >> 4) * 40960 + (t & 15) * 16) = z; }
  STAMP(44);
  v4u e[5]; u16 dvr[3];
  const int sl = act ? slot : 0;
  dvr[0] = dis[sl * 40 + (lane >> 2)]; dvr[1] = dis[sl * 40 + 16 + (lane >> 2)]; dvr[2] = dis[sl * 40 + 32 + ((lane & 31) >> 2)];
  const __amdgpu_buffer_rsrc_t rw = __builtin_amdgcn_make_buffer_rsrc(ws + WS_ELL, 0, WS_W - WS_ELL, 0x00020000);
  v2u hd = __builtin_amdgcn_raw_buffer_load_b64(rw, 0, WS_HDR - WS_ELL + sl * 8, 16);
#pragma unroll
  for (int i = 0; i < 5; i++) e[i] = __builtin_amdgcn_raw_buffer_load_b128(rw, lane * 16 + i * 1024, sl * 5120, ELLAUX);
  for (u32 spins = 0;; spins++) {
    u32 m = max(hd[0], hd[1]);
#pragma unroll
    for (int i = 0; i < 5; i++) m = max(max(m, max(e[i][0], e[i][1])), max(e[i][2], e[i][3]));
    if (__builtin_amdgcn_ballot_w64(m == POISON) == 0 || spins >= MAXSPIN) break;
    __builtin_amdgcn_s_sleep(2);
    asm volatile("" ::: "memory");
    hd = __builtin_amdgcn_raw_buffer_load_b64(rw, 0, WS_HDR - WS_ELL + sl * 8, 16);
#pragma unroll
    for (int i = 0; i < 5; i++) e[i] = __builtin_amdgcn_raw_buffer_load_b128(rw, lane * 16 + i * 1024, sl * 5120, 16);
  }
  const u32 novf = act ? min((u32)__builtin_amdgcn_readfirstlane(hd[1]), (u32)EE) : 0u;
  const u32 ovb = WS_OVF2 - WS_ELL + min((u32)__builtin_amdgcn_readfirstlane(hd[0]), (u32)EE) * 4;
  STAMP(45);
  const u32 vp01 = act ? slot * 128 + (lane >> 1) * 4 : OOB;
  const u32 vp2 = act ? 65536 + slot * 64 + (lane >> 1) * 4 : OOB;
  v8bf sel1, sel2; const int seli = (lane & 1) ? 0xEEEEEEEE : 0x44444444;
  {
    const u32 on = (lane & 2) ? 0u : 0x3f803f80u, lo = (lane & 4) ? 0u : on, hi = (lane & 4) ? on : 0u;
    const bool m1 = ((lane >> 4) & 1) == ((lane >> 3) & 1);
    const v4u a2 = {lo, lo, hi, hi}, a1 = {m1 ? lo : 0u, m1 ? lo : 0u, m1 ? hi : 0u, m1 ? hi : 0u};
    sel1 = __builtin_bit_cast(v8bf, a1); sel2 = __builtin_bit_cast(v8bf, a2);
  }
  const u32 vpz = w < 10 ? (w >= 5 ? GSTRIDE : 0u) + ((w % 5) >> 1) * 32768 + p * (w % 5 < 4 ? 2048 : 1024) + ((w % 5) & 1) * 1024 + lane * 16 : OOB;
  u32 vchk;
  {
    const u32 i = (lane >> 3) & 3, ln = (w == 15 && (i & 1)) ? (lane & 1) : (lane & 7);
    const u32 hl = (w == 15 && (lane & 15) >= 10) ? (lane & 7) : (lane & 15);
    vchk = lane < 32 ? (i == 2 ? lb2 : lb0 + (i == 1 ? 16384 : (i == 3 ? 90112 : 0))) + ln * 128 + lane * 4 : lb0 + 106496 + hl * 64 + (lane & 15) * 4;
  }
  u32 ad[32];
#pragma unroll
  for (int i = 0; i < 16; i++) { const u32 q = e[i >> 2][i & 3]; ad[2 * i] = (q & 0xffffu) * 8; ad[2 * i + 1] = (q >> 16) * 8; }
  u32 ex[4] = {e[4][0], e[4][1], e[4][2], e[4][3]};
  bool xm;
  {
    const u32 m0 = min(min(ex[0] & 0xffffu, ex[0] >> 16), min(ex[1] & 0xffffu, ex[1] >> 16));
    const u32 m1 = min(min(ex[2] & 0xffffu, ex[2] >> 16), min(ex[3] & 0xffffu, ex[3] >> 16));
    xm = __builtin_amdgcn_ballot_w64(min(m0, m1) < ZROW) != 0;
  }
  float dv2[3];
#pragma unroll
  for (int i = 0; i < 3; i++) { const float d = bf2f(dvr[i]); dv2[i] = d * d; }
  LBAR();
  STAMP(1);

  auto slow = [&](const u32 tofs, const u32 n0, const u32 nn) __attribute__((always_inline)) -> float {
    float a = 0.f;
    for (u32 base = 0; base < novf; base += 64) {
      const u32 en = base + lane < novf ? __builtin_amdgcn_raw_buffer_load_b32(rw, (base + lane) * 4, ovb, 16) : POISON;
      u64 m = __builtin_amdgcn_ballot_w64((en >> 16) - n0 < nn);
      while (m) {
        const int l = __builtin_ctzll(m); m &= m - 1;
        const u32 es = __builtin_amdgcn_readlane(en, l);
        const float val = bf2f(*(LAS u16*)(size_t)((es & 0xffffu) * 8 + tofs + (lane & 3) * 2));
        a += ((es >> 16) - n0 == (u32)(lane >> 2)) ? val : 0.f;
      }
    }
    return a;
  };
  auto grow = [&](const u32 la, const u32 scur) __attribute__((always_inline)) -> v2u {
    v2u v = {0, 0};
    if (la < ZROW * 8) {
      u32 spins = 0;
      do {
        v = __builtin_amdgcn_raw_buffer_load_b64(rs, la >= 81920 ? la - 40960 : la, scur, 16);
        asm volatile("" ::: "memory");
      } while (max(v[0], v[1]) == POISON && ++spins < MAXSPIN);
    }
    return v;
  };
  auto fallback = [&](const int bt, const u32 scur) __attribute__((always_inline)) -> float {
    float s0 = 0.f, s1 = 0.f, s2 = 0.f, s3 = 0.f;
#pragma unroll 1
    for (int j = 0; j < (bt < 2 ? 16 : 8); j++) {
      const int je = j < 12 ? bt * 12 + j : 20 + bt * 4 + j;
      const u32 la = __builtin_amdgcn_raw_buffer_load_b16(rw, (je >> 3) * 1024 + lane * 16 + (je & 7) * 2, sl * 5120, 16) * 8u;
      const v2u v = grow(la, scur);
      s0 += bflo(v[0]); s1 += bfhi(v[0]); s2 += bflo(v[1]); s3 += bfhi(v[1]);
    }
    float a = 0.f;
#pragma unroll
    for (int c = 0; c < 4; c++) {
      const int sl_ = (lane & 48) + 4 * c + ((lane >> 2) & 3);
      const float t0 = __shfl(s0, sl_), t1 = __shfl(s1, sl_), t2 = __shfl(s2, sl_), t3 = __shfl(s3, sl_);
      a += (lane & 2) ? ((lane & 1) ? t3 : t2) : ((lane & 1) ? t1 : t0);
    }
    if (bt == 2) a += __shfl_xor(a, 32);
    const u32 n0 = slot * 40 + bt * 16;
    for (u32 i = 0; i < novf; i++) {
      const u32 es = __builtin_amdgcn_raw_buffer_load_b32(rw, i * 4, ovb, 16);
      if ((es >> 16) - n0 == (u32)(lane >> 2) && (bt < 2 || lane < 32)) {
        const v2u v = grow((es & 0xffffu) * 8, scur);
        const u32 q = (lane & 2) ? v[1] : v[0];
        a += (lane & 1) ? bfhi(q) : bflo(q);
      }
    }
    return a;
  };
  auto tail = [&](const int bt, const v4f acc, const u32 scur, const u32 voff, const u32 soff) __attribute__((always_inline)) {
    float a = acc[0] + acc[1];
    if (__builtin_expect(__builtin_amdgcn_ballot_w64(a != a) != 0, 0)) a = fallback(bt, scur);
    const float d2 = dv2[bt];
    const float tn = __builtin_bit_cast(float, DPPU(__float_as_uint(a), 0xB1)) * d2;
    const u32 cm = min(pk2(a * d2, tn), 0xFFFFFFFEu);
    asm volatile("s_mov_b64 exec, %4\n\tbuffer_store_dword %0, %1, %2, %3 offen sc1\n\ts_mov_b64 exec, -1"
                 :: "v"(cm), "v"(voff), "s"(srd), "s"(soff), "s"(bt < 2 ? 0x5555555555555555ull : 0x55555555ull) : "memory");
  };
  v4f acc = {0, 0, 0, 0}, acc2 = {0, 0, 0, 0};
  u32 vp2x = OOB;
  auto phase = [&](auto GBc, auto XMc, const bool recv, const u32 sprev, const u32 scur, const u32 srecv_, const int sb) __attribute__((always_inline)) {
    const u32 srecv = srecv_;
    constexpr u32 GB = decltype(GBc)::value;
    constexpr bool XM = decltype(XMc)::value;
    constexpr u32 to = GB * 40960u, ro = (1 - GB) * 40960u;
    constexpr int NQ = XM ? 10 : 8, E0 = XM ? 3 : 2, E1 = XM ? 7 : 5;
    const bool pois = GB == 0 && scur < sgA + (LASTP - 2) * GBUF;
    __builtin_amdgcn_s_setprio(3);
    if (recv) { DMA(vla, srecv, lb0 + ro); DMA(vla, srecv + 16384, lb0 + 16384 + ro); }
    auto grp = [&](const int s) __attribute__((always_inline)) -> v16bf {
      if (XM && (s == E0 || s == E1)) {
        u32 q0 = ex[s == E0 ? 0 : 2], q1 = ex[s == E0 ? 1 : 3];
        asm volatile("" : "+v"(q0), "+v"(q1));
        return rd4((q0 & 0xffffu) * 8 + to, (q0 >> 16) * 8 + to, (q1 & 0xffffu) * 8 + to, (q1 >> 16) * 8 + to);
      }
      const int q = XM ? s - (s > E0) - (s > E1) : s;
      return rd4(ad[4 * q] + to, ad[4 * q + 1] + to, ad[4 * q + 2] + to, ad[4 * q + 3] + to);
    };
    v16bf g[3];
#pragma unroll
    for (int q = 0; q < 3; q++) g[q] = grp(q);
    __builtin_amdgcn_sched_barrier(0);
    if (D23POS < 0 && recv) { DMA(vla, srecv + 32768, lb2 + ro); DMA(vla, srecv + 49152, lb0 + 90112 + ro); }
    float e0 = 0.f, e1 = 0.f, e2 = 0.f;
    if (__builtin_expect(novf != 0, 0)) { e0 = slow(to, slot * 40, 16); e1 = slow(to, slot * 40 + 16, 16); e2 = slow(to, slot * 40 + 32, 8); }
    __builtin_amdgcn_sched_barrier(0);
    u32 chk = 0;
#pragma unroll
    for (int q = 0; q < NQ; q++) {
      if (q == 0) { acc[0] = e0; acc[1] = 0.f; }
      if (q == E0 + 1) { acc[0] = e1; acc[1] = 0.f; }
      if (q == E1 + 1) { acc2[0] = e2; acc2[1] = 0.f; }
      if (q == NQ - 1 && recv) {
        asm volatile("s_waitcnt vmcnt(%0)" :: "n"(D4POS <= E0 ? 2 : 1) : "memory");
        chk = *(volatile LAS u32*)(size_t)(vchk + ro);
      }
      if (q <= E1) acc = __builtin_amdgcn_smfmac_f32_16x16x64_bf16(sel1, g[q % 3], acc, seli, 0, 0);
      else acc2 = __builtin_amdgcn_smfmac_f32_16x16x64_bf16(sel2, g[q % 3], acc2, seli, 0, 0);
      if (q + 3 < NQ) g[q % 3] = grp(q + 3);
      if (q == D23POS && recv) { DMA(vla, srecv + 32768, lb2 + ro); DMA(vla, srecv + 49152, lb0 + 90112 + ro); }
      if (q == D4POS && recv) DMA(vlc, srecv + 65536, lb0 + 106496 + ro);
      if (q == PR1) __builtin_amdgcn_s_setprio(2);
      if (q == PR2) __builtin_amdgcn_s_setprio(1);
      if (q == PR3) __builtin_amdgcn_s_setprio(0);
      __builtin_amdgcn_sched_barrier(0);
      if (q == E0 || q == E1) {
        tail(q == E1, acc, scur, vp01, scur + GBUF + (q == E1 ? 32768 : 0));
        __builtin_amdgcn_sched_barrier(0);
      }
    }
    tail(2, acc2, scur, vp2, scur + GBUF);
      if (pois) {
        const v4u pz = {POISON, POISON, POISON, POISON};
        asm volatile("buffer_store_dwordx4 %0, %1, %2, %3 offen sc1" :: "v"(pz), "v"(vpz), "s"(srd), "s"(scur + 3 * GBUF) : "memory");
      }

    PSTAMP(sb);
    if (recv) {
      u32 spins = 0;
      u64 bad = __builtin_amdgcn_ballot_w64(chk == POISON);
      if (__builtin_expect(bad != 0, 0)) {
        do {
          spins++;
          if (bad & 0xffull) DMA(vla, srecv, lb0 + ro);
          if (bad & 0xff00ull) DMA(vla, srecv + 16384, lb0 + 16384 + ro);
          if (bad & 0xff0000ull) DMA(vla, srecv + 32768, lb2 + ro);
          if (bad & 0xff000000ull) DMA(vla, srecv + 49152, lb0 + 90112 + ro);
          if (bad >> 32) DMA(vlc, srecv + 65536, lb0 + 106496 + ro);
          asm volatile("s_waitcnt vmcnt(0)" ::: "memory");
          bad = __builtin_amdgcn_ballot_w64(*(volatile LAS u32*)(size_t)(vchk + ro) == POISON);
        } while (bad != 0 && spins < MAXSPIN);
      }
      PSTAMP(sb + 1);
      asm volatile("" ::: "memory");
      __builtin_amdgcn_s_barrier();
      asm volatile("" ::: "memory");
      PSTAMP(sb + 2);
    }
  };
  using F0 = std::integral_constant<u32, 0>; using F1 = std::integral_constant<u32, 1>;
  auto hops = [&](auto XMc) __attribute__((always_inline)) {
    u32 sk = sgA;
#pragma unroll 1
    for (int k = 1; k <= KMAX; k++) {
      phase(F0{}, XMc, true, sk - GBUF + GSTRIDE, sk, sk + GSTRIDE, 6 * k - 4);
      phase(F1{}, XMc, k < KMAX, sk, sk + GSTRIDE, sk + GBUF, 6 * k - 1);
      sk += GBUF;
    }
  };
  if (xm) hops(std::true_type{}); else hops(std::false_type{});
  STAMP(47);
  STAMP_END;
#undef KID
}

#define L_WR 32768u
#define L_WW 81920u
__device__ __forceinline__ u32 chunk(u32 id) { return 40960u + id * 2048u + (id >= 20u ? 40960u : 0u); }
__device__ __forceinline__ v4u ld128(u32 a) { return *(const LAS v4u*)(size_t)a; }
__device__ __forceinline__ v2u ld64(u32 a) { return *(const LAS v2u*)(size_t)a; }
__device__ __forceinline__ float bperm(u32 a, float v) { return __uint_as_float((u32)__builtin_amdgcn_ds_bpermute((int)a, (int)__float_as_uint(v))); }
__device__ __forceinline__ float dot2(u32 a, u32 b, float c) { return __builtin_amdgcn_fdot2_f32_bf16(__builtin_bit_cast(v2bf, a), __builtin_bit_cast(v2bf, b), c, false); }
#define RD 3
#define SB() __builtin_amdgcn_sched_barrier(0)
template <int I, int E, class F> __device__ __forceinline__ void sfor(F&& fn) {
  if constexpr (I < E) { fn(std::integral_constant<int, I>{}); sfor<I + 1, E>(fn); }
}
#define fmx(a, b) __builtin_amdgcn_fmed3f((a), (b), finf)
#define PS1 24
#define PS2 48
#define FO(mm) ((mm) == 0 ? 39 : 38 + 4 * (mm) - ((mm) == 7))
#define OPQ(x) asm volatile("" : "+v"(x))
#define OPQ2(x, y) asm volatile("" : "+v"(x), "+v"(y))
__device__ __forceinline__ float sel(bool c, float a, float b) { asm("" : "+v"(a)); return c ? a : b; }
__device__ __forceinline__ u32 pk2o(float a, float b) { asm("" : "+v"(a)); asm("" : "+v"(b)); return pk2(a, b); }
struct TfPre { u32 aT, aR, oo, bp, bq; };
__device__ __forceinline__ TfPre tfpre(const u32 j0, const u32 slot, const u32 lane) {
  const u32 g = lane >> 4, n = lane & 15, k = n >> 1, nd = n & 1, u = j0 >> 2, kr = 2 * g + ((n >> 2) & 1);
  TfPre p;
  p.aT = chunk(u * 8 + k) + (g ^ ((k >> 2) << 1)) * 64 + ((j0 ^ k) & 3) * 16 + nd * 8;
  p.aR = chunk(u * 8 + kr) + ((n & 3) ^ ((kr >> 2) << 1)) * 64 + ((j0 ^ kr) & 3) * 16 + (n >> 3) * 8;
  p.oo = (slot * 40 + 2 * j0 + nd) * 256 + k * 32 + 8 * g;
  p.bp = (20 * g + nd) * 4; p.bq = (16 * (n >> 2) + n) * 4;
  OPQ2(p.aT, p.aR); OPQ2(p.bp, p.bq); OPQ(p.oo);
  return p;
}
template <int NT, int PQ>
__device__ __forceinline__ void tf(const TfPre& P, const u32 lane, const float dv0, const float dv1, u16* __restrict__ out) {
  const u32 g = lane >> 4, n = lane & 15, k = n >> 1, nd = n & 1;
  const u32 aT = P.aT, bp = P.bp, bq = P.bq, oo = P.oo;
  u32 aW = lane * 16;
  float finf = __builtin_inff(); asm("" : "+s"(finf));
  v4u TF[NT][4], W[RD];
#pragma unroll
  for (int s = 0; s < 4; s++) {
#pragma unroll
    for (int t = 0; t < NT; t++) {
      const v2u a = ld64((aT ^ (16 * t)) + 512 * s), b = ld64((aT ^ (16 * t)) + 512 * s + 256);
      TF[t][s] = v4u{a[0], a[1], b[0], b[1]};
    }
    if (s < RD) W[s] = ld128(aW + L_WR + s * 1024);
  }
  SB();
  u32 aR[2] = {P.aR, P.aR ^ 16};
  OPQ(aR[1]);
  const float dve[2] = {dv0, dv1};
  float rho[NT], rb[NT][2], c1[NT][2], c3[NT][2], v0[NT], v1[NT], mx[2], zs[2], l1[2], l3[2], q2[2][2];
  v4f S[NT], LN[NT], GR[NT], U[NT][8], y[NT][8], o[NT][8];
  v4u ub[NT], B[NT]; v2u Y[NT][8], tr[NT][8]; u32 dg0[NT], dg1[NT];
  const v4f Z4 = {0, 0, 0, 0};
  SB();
  sfor<-4, 68>([&](auto Fc) __attribute__((always_inline)) {
    constexpr int f = decltype(Fc)::value, m = (f >> 2) & 7, s = f & 3, fn = f + RD;
    if constexpr (f == -4) __builtin_amdgcn_s_setprio(PQ & 3);
    if constexpr (f == PS1) __builtin_amdgcn_s_setprio((PQ >> 4) & 3);
    if constexpr (f == PS2) __builtin_amdgcn_s_setprio((PQ >> 8) & 3);
    if constexpr (f >= 10 && f <= 34 && (f & 7) == 2) {
#pragma unroll
      for (int t = 0; t < NT; t++) S[t] = MFMA(TF[t][(f - 10) >> 3], ub[t], (f > 10 ? S[t] : Z4));
      SB();
    }
#pragma unroll
    for (int mm = 0; mm < 8; mm++) {
      if (f == FO(mm)) {
#pragma unroll
        for (int t = 0; t < NT; t++) o[t][mm] = MFMA((v4u{tr[t][mm][0], tr[t][mm][1], Y[t][mm][0], Y[t][mm][1]}), B[t], Z4);
        SB();
      }
    }
    if constexpr (f < 64) {
      const v4u wf = W[(f + 4) % RD];
#pragma unroll
      for (int t = 0; t < NT; t++) {
        if constexpr (f < 0) { GR[t] = MFMA(TF[t][s], TF[t][s], (s ? GR[t] : Z4)); LN[t] = MFMA(wf, TF[t][s], (s ? LN[t] : Z4)); }
        else if constexpr (f < 32) U[t][m] = MFMA(wf, TF[t][s], (s ? U[t][m] : Z4));
        else y[t][m] = MFMA(TF[t][s], wf, (s ? y[t][m] : Z4));
      }
      if constexpr (fn == 32) { aW += L_WW; OPQ(aW); }
      if constexpr (fn < 64)
      W[(f + 4) % RD] = ld128(aW + (fn < 0 ? L_WR + (fn + 4) * 1024 : (fn & 31) * 1024));
    }
#pragma unroll
    for (int t = 0; t < NT; t++) {
      if (f == 1 + 2 * t) {
        const float qa = sel(nd, GR[t][1], GR[t][0]), qb = sel(nd, GR[t][3], GR[t][2]), q = (n & 2) ? qb : qa;
        const float a = LN[t][0] * (1.f / 128.f);
        rho[t] = fmaxf(q * (1.f / 128.f) - a * a, 0.f) + (k == 0 ? 1e-5f : 1e-5f * dve[t] * dve[t]);
        OPQ(rho[t]);
      }
      if (f == 2 + 2 * t) {
        rho[t] = __builtin_amdgcn_rsqf(rho[t]); rho[t] = k == 0 ? rho[t] : __builtin_copysignf(rho[t], dve[t]);
        const float dd = g < 2 ? (k == 0 ? dve[t] : 1.f) : 0.f;
        l1[t] = 0.5f * dd * LN[t][0]; l3[t] = dd * LN[t][1];
        OPQ2(l1[t], l3[t]);
      }
      if (f == 6 + t) {
        rb[t][0] = bperm(bp, rho[t]); rb[t][1] = bperm(bp + 8, rho[t]); rho[t] = bperm(bq, rho[t]);
      }
      if (f == 8 + t) {
        const u32 d1 = k == 0 ? __float_as_uint(dve[t]) : 0x3f800000u, d2 = (k >> 1) == g ? d1 : 0u, dg = d2 >> ((n & 2) ? 0 : 16);
        dg0[t] = nd ? 0u : dg; dg1[t] = nd ? dg : 0u; OPQ2(dg0[t], dg1[t]);
      }
      if (f == 41) { c1[t][0] = bperm(bp, LN[t][2]); c1[t][1] = bperm(bp + 8, LN[t][2]); c3[t][0] = bperm(bp, LN[t][3]); c3[t][1] = bperm(bp + 8, LN[t][3]); }
      if (f == 12) { v0[t] = rb[t][0] * rho[t]; v1[t] = rb[t][1] * rho[t]; OPQ2(v0[t], v1[t]); rb[t][0] *= dve[t]; rb[t][1] *= dve[t]; OPQ2(rb[t][0], rb[t][1]); }
    }
    if constexpr (f >= 5 && f <= 33 && s == 1) {
      constexpr int mm = (f - 5) >> 2;
#pragma unroll
      for (int t = 0; t < NT; t++) { ub[t][(mm & 1) * 2] = pk2(U[t][mm][0], U[t][mm][1]); ub[t][(mm & 1) * 2 + 1] = pk2(U[t][mm][2], U[t][mm][3]); }
    }
    if (f == 35) {
#pragma unroll
      for (int t = 0; t < NT; t++) { v0[t] *= sel(nd, S[t][1], S[t][0]); v1[t] *= sel(nd, S[t][3], S[t][2]); OPQ2(v0[t], v1[t]); mx[t] = fmx(v0[t], v1[t]); }
      if (NT == 1) mx[1] = mx[0];
      sw16(mx[0], mx[1]); mx[0] = fmx(mx[0], mx[1]);
    }
    if (f == 36) { mx[1] = mx[0]; sw32(mx[0], mx[1]); mx[0] = fmx(mx[0], mx[1]); mx[1] = mx[0]; if (NT == 2) sw16(mx[0], mx[1]); }
    if (f == 36) {
#pragma unroll
      for (int t = 0; t < NT; t++) { v0[t] = __builtin_amdgcn_exp2f(v0[t] - mx[t]); v1[t] = __builtin_amdgcn_exp2f(v1[t] - mx[t]); zs[t] = v0[t] + v1[t]; }
      if (NT == 1) zs[1] = zs[0];
    }
    if (f == 37) { sw16(zs[0], zs[1]); zs[0] += zs[1]; zs[1] = zs[0]; sw32(zs[0], zs[1]); zs[0] = __builtin_amdgcn_rcpf(zs[0] + zs[1]); zs[1] = zs[0]; if (NT == 2) sw16(zs[0], zs[1]); }
    if (f == 38) {
#pragma unroll
      for (int t = 0; t < NT; t++) {
        v0[t] *= rb[t][0]; v1[t] *= rb[t][1]; OPQ(v0[t]); v0[t] *= zs[t]; v1[t] *= zs[t];
        const u32 p = pk2o(v0[t], v1[t]);
        B[t] = v4u{dg0[t], dg1[t], nd ? 0u : p, nd ? p : 0u};
      }
    }
    if (f == 44) {
#pragma unroll
      for (int t = 0; t < NT; t++) {
        l1[t] += v0[t] * c1[t][0]; l3[t] += v0[t] * c3[t][0]; OPQ2(l1[t], l3[t]); l1[t] += v1[t] * c1[t][1]; l3[t] += v1[t] * c3[t][1];
        OPQ2(l1[t], l3[t]);
      }
    }
    if (f == 48) {
#pragma unroll
      for (int t = 0; t < NT; t++) { sw32(l1[t], l3[t]); l1[t] += l3[t]; }
      if (NT == 1) l1[1] = l1[0];
      sw16(l1[0], l1[1]); l1[0] += l1[1];
    }
    if (f == 52) { l3[0] = l1[0]; sw32(l1[0], l3[0]); l1[0] *= 1.f / 128.f; OPQ2(l1[0], l3[0]); }
#pragma unroll
    for (int mm = 0; mm < 8; mm++) {
      const int fo = FO(mm);
      if (f == fo - 3) {
#pragma unroll
        for (int t = 0; t < NT; t++) tr[t][mm] = __builtin_bit_cast(v2u, rd(aR[t] + 256 * mm));
      }
      if (f == 37 + 4 * mm - (mm == 7)) {
#pragma unroll
        for (int t = 0; t < NT; t++) Y[t][mm] = v2u{pk2(y[t][mm][0], y[t][mm][2]), pk2(y[t][mm][1], y[t][mm][3])};
      }
#pragma unroll
      for (int h = 0; h < 2; h++) {
        if (f == fo + 1 + h) {
#pragma unroll
          for (int t = 0; t < NT; t++) {
            const float a = o[t][mm][2 * h], c = o[t][mm][2 * h + 1];
            if (mm == 0 && h == 0) { q2[t][0] = a * a; q2[t][1] = c * c; } else { q2[t][0] += a * a; q2[t][1] += c * c; }
            OPQ2(q2[t][0], q2[t][1]);
          }
        }
      }
    }
    SB();
  });
#pragma unroll
  for (int m = 0; m < 8; m++)
#pragma unroll
    for (int t = 0; t < NT; t++) tr[t][m] = __builtin_bit_cast(v2u, rd(aR[t] + 256 * m));
  const float dr = (NT == 2 && (g & 1)) ? dve[1] : dve[0];
  float al;
  {
    float qa = q2[0][0] + q2[0][1], qb = q2[NT - 1][0] + q2[NT - 1][1];
    sw16(qa, qb); qa += qb; qb = qa; sw32(qa, qb);
    const float var = fmaxf((qa + qb) * (1.f / 128.f) - l1[0] * l1[0], 0.f);
    const float lg = l3[0] * __builtin_copysignf(__builtin_amdgcn_rsqf(var + 1e-5f * dr * dr), dr);
    float mm = fmx(lg, DPPF(lg, 0x122)); mm = fmx(mm, DPPF(mm, 0x124)); mm = fmx(mm, DPPF(mm, 0x128));
    const float e = __builtin_amdgcn_exp2f(lg - mm);
    float z = e + DPPF(e, 0x122); z += DPPF(z, 0x124); z += DPPF(z, 0x128);
    al = e * __builtin_amdgcn_rcpf(z);
  }
  const float ald = al * __builtin_amdgcn_rcpf(dr), als = k == 0 ? al : ald;
  float aa[2] = {ald, ald}; if (NT == 2) sw16(aa[0], aa[1]);
  const u32 br = (4 * g + nd) * 4;
  v4f R[NT][2];
#pragma unroll
  for (int t = 0; t < NT; t++) {
    const float a0 = bperm(br + 64 * t, als), a1 = bperm(br + 64 * t + 8, als);
    const u32 p = B[t][2] | B[t][3];
    float b0 = bflo(p) * aa[t], b1 = bfhi(p) * aa[t];
    OPQ(b0);
    b0 += DPPF(b0, 0x122); b1 += DPPF(b1, 0x122); b0 += DPPF(b0, 0x124); b1 += DPPF(b1, 0x124); b0 += DPPF(b0, 0x128); b1 += DPPF(b1, 0x128);
    const u32 xa = pk2o(a0, a1), xb = pk2o(b0, b1);
#pragma unroll
    for (int c = 0; c < 4; c++) {
      const bool e0 = (n & 13) == 4 * c, e1 = (n & 13) == 4 * c + 1;
      const v4u gb = {e0 ? xa : 0u, e1 ? xa : 0u, e0 ? xb : 0u, e1 ? xb : 0u};
#pragma unroll
      for (int j = 0; j < 2; j++) R[t][j] = MFMA((v4u{tr[t][2 * c + j][0], tr[t][2 * c + j][1], Y[t][2 * c + j][0], Y[t][2 * c + j][1]}), gb, (c ? R[t][j] : Z4));
    }
  }
#pragma unroll
  for (int t = 0; t < NT; t++) {
    const u32 r0 = pk2(R[t][0][0], R[t][0][1]), r1 = pk2(R[t][0][2], R[t][0][3]), r2 = pk2(R[t][1][0], R[t][1][1]), r3 = pk2(R[t][1][2], R[t][1][3]);
    __builtin_amdgcn_raw_buffer_store_b64(v2u{(n & 2) ? r2 : r0, (n & 2) ? r3 : r1}, __builtin_amdgcn_make_buffer_rsrc(out, 0, NN * 256, 0x00020000), oo + 512 * t, 0, 0);
  }
}
#define P3A 0x123
#define P3B 0x123
#define P3C 0x123

#define K3T 768
__global__ void __launch_bounds__(K3T) k3(const u16* __restrict__ x, const u16* __restrict__ dis, char* __restrict__ ws, u16* __restrict__ out) {
#define KID 3
  __shared__ __attribute__((aligned(16))) char T[163840];
  const int t = threadIdx.x, lane = t & 63, w = __builtin_amdgcn_readfirstlane(t >> 6), slot = blockIdx.x;
  asm volatile("" :: "v"((u32)(size_t)(LAS char*)T));
  STAMP_INIT STAMP(0);
  const v4u srd = {(u32)(size_t)ws, (u32)((size_t)ws >> 32) & 0xffffu, WS_DBG, 0x00020000u};
  v4u xv = {0, 0, 0, 0};
  if (t < 640) xv = *(const v4u*)(x + (size_t)slot * 5120 + t * 8);
  const u32 j0 = w < 8 ? 2 * w : min(w + 8, 19), two = w < 8 ? 2 : 0;
  const u16 dq0 = dis[slot * 40 + 2 * j0 + (lane & 1)], dq1 = dis[slot * 40 + 2 * j0 + two + (lane & 1)];
  for (int id = w; id < 70; id += K3T / 64) {
    const u32 k = id / 10 + 1, rem = id - (k - 1) * 10, u = rem >> 1, h = rem & 1;
    const u32 vo = ((lane >> 2) ^ ((k >> 2) << 1)) * GSTRIDE + ((lane ^ k) & 3) * 16;
    const u32 so = WS_G + k * GBUF + h * 16 * GSTRIDE + (u < 4 ? (u >> 1) * 32768 + 128 * slot + (u & 1) * 64 : 65536 + 64 * slot);
    DMA0(vo, so, chunk(u * 8 + k) + h * 1024);
  }
  for (int id = w; id < 68; id += K3T / 64) DMA0(lane * 16, WS_W + id * 1024, id < 32 ? id * 1024 : (id < 64 ? L_WW + (id - 32) * 1024 : L_WR + (id - 64) * 1024));
  if (t < 640) {
    const u32 node = t >> 4, a = chunk((node >> 3) * 8) + (t & 15) * 128 + ((node >> 1) & 3) * 16 + (node & 1) * 8;
    *(LAS v2u*)(size_t)a = v2u{xv[0], xv[1]}; *(LAS v2u*)(size_t)(a + 64) = v2u{xv[2], xv[3]};
  }
  const TfPre P = tfpre(j0, slot, lane);
  float dv0 = bf2f(dq0), dv1 = bf2f(dq1);
  dv0 = dv0 == 0.f ? 1.f : dv0; dv1 = dv1 == 0.f ? 1.f : dv1; OPQ2(dv0, dv1);
  asm volatile("" ::: "memory");
  __builtin_amdgcn_s_waitcnt(0x0070);
  __builtin_amdgcn_s_barrier();
  asm volatile("" ::: "memory");
  STAMP(1);
  if (w < 4) tf<2, P3A>(P, lane, dv0, dv1, out);
  else if (w < 8) tf<2, P3B>(P, lane, dv0, dv1, out);
  else if (w < 12) tf<1, P3C>(P, lane, dv0, dv1, out);
  STAMP(2);
  STAMP_END;
#undef KID
}
__device__ __host__ __forceinline__ u32 rowof(u32 n) { u32 s = n / 40, l = n - s * 40; return l < 32 ? (l >> 4) * 4096 + 16 * s + (l & 15) : 8192 - 32 + 8 * s + l; }

extern "C" void kernel_launch(void* const* d_in, const int* in_sizes, int n_in, void* d_out, int out_size, void* d_ws, size_t ws_size, hipStream_t stream) {
  const u16* x = (const u16*)d_in[0]; const int* src = (const int*)d_in[1]; const int* dst = (const int*)d_in[2]; const u16* dis = (const u16*)d_in[3];
  const u16* wq = (const u16*)d_in[4]; const u16* wk = (const u16*)d_in[5]; const u16* wv = (const u16*)d_in[6]; const u16* wo = (const u16*)d_in[7]; const u16* wr = (const u16*)d_in[8];
  char* ws = (char*)d_ws;
  k1<<<253, 640, 0, stream>>>(src, dst, x, dis, wq, wk, wv, wo, ws);
  k2<<<256, 1024, 0, stream>>>(dis, wq, wk, wv, wo, wr, ws);
  k3<<<250, K3T, 0, stream>>>(x, dis, ws, (u16*)d_out);
}
#define WS_TOK WS_G
#define TOKBYTES (8u * GBUF)
#define TOKSC(n) hbf2f(dis[n])
#define TOKADDR(n,k,d) ((size_t)(k) * GBUF + ((d) >> 2) * GSTRIDE + rowof(n) * 8 + ((d) & 3) * 2)
```

```cpp
#include <hip/hip_runtime.h>
#include <hip/hip_bf16.h>
#include <stdint.h>
#include <type_traits>

#define NN 10000
#define EE 320000
typedef unsigned short u16;
typedef unsigned u32;
typedef unsigned long long u64;
typedef u32 v4u __attribute__((ext_vector_type(4)));
typedef u32 v2u __attribute__((ext_vector_type(2)));
typedef short v4s __attribute__((ext_vector_type(4)));
typedef float v4f __attribute__((ext_vector_type(4)));
typedef __bf16 v4bf __attribute__((ext_vector_type(4)));
typedef __bf16 v8bf __attribute__((ext_vector_type(8)));
typedef __bf16 v16bf __attribute__((ext_vector_type(16)));
typedef __bf16 v2bf __attribute__((ext_vector_type(2)));
typedef float v2f __attribute__((ext_vector_type(2)));
typedef u16 v2h __attribute__((ext_vector_type(2)));
#define LAS __attribute__((address_space(3)))

#define GSTRIDE 81920u
#define GBUF    (32u * GSTRIDE)
#define WS_G    0u
#define WS_SUB  (8u * GBUF)
#define WS_ELL  (WS_SUB + 4096000u)
#define WS_HDR  (WS_ELL + 1280000u)
#define WS_FLG  (WS_HDR + 2048u)
#define WS_CNT  (WS_FLG + 1024u)
#define WS_OVF1 (WS_CNT + 256u)
#define WS_OVF2 (WS_OVF1 + 128u * 2504u * 4u)
#define WS_W    (WS_OVF2 + 1280000u)
#define WS_DBG  (WS_W + 69632u)
#define POISON 0xFFFFFFFFu
#define OOB 0x40000000u
#define ZROW 15312u
#define MAXSPIN 100000u
#define AUX1 16

#define STAMP_INIT
#define STAMP(i)
#define STAMP_END

#define WST
#define WSA
#define PSTAMP(i) STAMP(i)
__device__ __forceinline__ float bf2f(u16 v) { return __uint_as_float(((u32)v) << 16); }
__device__ __forceinline__ float bflo(u32 v) { return __uint_as_float(v << 16); }
__device__ __forceinline__ float bfhi(u32 v) { return __uint_as_float(v & 0xffff0000u); }
__device__ __forceinline__ u32 pk2(float a, float b) { v2f f = {a, b}; v2bf r = __builtin_convertvector(f, v2bf); return __builtin_bit_cast(u32, r); }
__device__ __forceinline__ u32 pk2s(float a, float b) { return min(pk2(a, b), 0xFFFFFFFEu); }
#define DPPU(v, ctrl) ((u32)__builtin_amdgcn_mov_dpp((int)(v), (ctrl), 0xF, 0xF, true))
#define LBAR() do { asm volatile("" ::: "memory"); __builtin_amdgcn_s_waitcnt(0xC07F); __builtin_amdgcn_s_barrier(); asm volatile("" ::: "memory"); } while (0)
__device__ __forceinline__ u32 tabidx(u32 n) {
  u32 s = (n * 52429u) >> 21, l = n - s * 40u;
  u32 row = l < 32u ? ((l >> 4) << 12) + 16u * s + (l & 15u) : 8192u - 32u + 8u * s + l;
  return row + (row >= 5120u ? 5120u : 0u);
}

#define DMA0(voff, soff, lbase) asm volatile("s_mov_b32 m0, %2\n\ts_nop 0\n\tbuffer_load_dwordx4 %0, %3, %1 offen lds" :: "v"(voff), "s"(soff), "s"(lbase), "s"(srd) : "memory")
__device__ __forceinline__ v4bf rd(u32 a) { return __builtin_amdgcn_ds_read_tr16_b64_v4bf16((LAS v4bf*)(size_t)a); }
#define MFMA(a, b, c) __builtin_amdgcn_mfma_f32_16x16x32_bf16(__builtin_bit_cast(v8bf, (v4u)(a)), __builtin_bit_cast(v8bf, (v4u)(b)), (c), 0, 0, 0)
__device__ __forceinline__ void sw16(float& a, float& b) { const v2u r = __builtin_amdgcn_permlane16_swap(__float_as_uint(a), __float_as_uint(b), false, false); a = __uint_as_float(r[0]); b = __uint_as_float(r[1]); }
__device__ __forceinline__ void sw32(float& a, float& b) { const v2u r = __builtin_amdgcn_permlane32_swap(__float_as_uint(a), __float_as_uint(b), false, false); a = __uint_as_float(r[0]); b = __uint_as_float(r[1]); }
#define DPPF(v, ctrl) __uint_as_float(DPPU(__float_as_uint(v), ctrl))
#define LOG2E 1.4426950408889634f
template <class F>
__device__ __forceinline__ void wprep(const int which, const u16* wq, const u16* wk, const u16* wv, const u16* wo, const u16* wr, char* ws, float* L, F&& early WST) {
  const int t = threadIdx.x, lane = t & 63, m = __builtin_amdgcn_readfirstlane(t >> 6), g = lane >> 4, i = lane & 15;
  v4f D[8];
  v4u B[4];
  const __amdgpu_buffer_rsrc_t rsw = __builtin_amdgcn_make_buffer_rsrc(ws + WS_W, 0, 69632, 0x00020000);
  const float wra = bf2f(wr[lane]), wrb = bf2f(wr[64 + lane]), wrm = bf2f(wr[16 * (m & 7) + i]);
  const u32 lb = (u32)(size_t)(LAS float*)L;
  if (m < 8) {
    const u16* wa = which == 0 ? wq : wv;
    {
      const v4u srd = {(u32)(size_t)wa, (u32)((size_t)wa >> 32) & 0xffffu, 32768u, 0x00020000u};
#pragma unroll
      for (int ks = 0; ks < 4; ks++) DMA0(i * 256 + g * 16, m * 4096 + ks * 64, lb + (m * 4 + ks) * 1024);
    }
    if (which == 0) {
#pragma unroll
      for (int ks = 0; ks < 4; ks++) B[ks] = *(const v4u*)(wk + (16 * m + i) * 128 + 32 * ks + 8 * g);
    } else {
      const v4u srd = {(u32)(size_t)wo, (u32)((size_t)wo >> 32) & 0xffffu, 32768u, 0x00020000u};
#pragma unroll
      for (int ks = 0; ks < 4; ks++) DMA0(lane * 16, (m * 4 + ks) * 1024, lb + 81920 + (m * 4 + ks) * 1024);
    }
  }
  asm volatile("" ::: "memory");
  __builtin_amdgcn_s_waitcnt(0x0070);
  __builtin_amdgcn_s_barrier();
  asm volatile("" ::: "memory");
  early();
  STAMP(40);
  if (m < 8) {
    if (which == 1) {
#pragma unroll
      for (int ks = 0; ks < 4; ks++) {
        const u32 a = lb + 81920 + (32 * ks + 8 * g + (i >> 2)) * 256 + m * 32 + (i & 3) * 8;
        const v2u lo = __builtin_bit_cast(v2u, rd(a)), hi = __builtin_bit_cast(v2u, rd(a + 1024));
        B[ks] = v4u{lo[0], lo[1], hi[0], hi[1]};
      }
    }
    float cs = 0.f;
#pragma unroll
    for (int dt = 0; dt < 8; dt++) {
      v4f a = {0, 0, 0, 0};
#pragma unroll
      for (int ks = 0; ks < 4; ks++) a = MFMA(*(const LAS v4u*)(size_t)(lb + (dt * 4 + ks) * 1024 + lane * 16), B[ks], a);
      D[dt] = a; cs += (a[0] + a[1]) + (a[2] + a[3]);
    }
    STAMP(41);
    float c2 = cs; sw16(cs, c2); cs += c2; c2 = cs; sw32(cs, c2); cs = (cs + c2) * (1.f / 128.f);
#pragma unroll
    for (int dt = 0; dt < 8; dt++) D[dt] -= cs;
    float wc = 0.f;
    if (which == 1) {
      float z = wra + wrb; z += DPPF(z, 0x121); z += DPPF(z, 0x122); z += DPPF(z, 0x124); z += DPPF(z, 0x128);
      c2 = z; sw16(z, c2); z += c2; c2 = z; sw32(z, c2); z = (z + c2) * (1.f / 128.f);
      wc = (wrm - z) * LOG2E;
      if (g == 0) L[28672 + 16 * m + i] = wc;
    }
#pragma unroll
    for (int pass = 0; pass < 2; pass++) {
      if (pass == 0 || which == 1) {
        v4f r[8];
#pragma unroll
        for (int dt = 0; dt < 8; dt++) r[dt] = pass ? D[dt] * wc : D[dt];
        __builtin_amdgcn_sched_barrier(0);
#pragma unroll
        for (int st = 0; st < 4; st++) {
#pragma unroll
          for (int dt = 0; dt < 8; dt++)
#pragma unroll
            for (int q = 0; q < 4; q++) { const float z = r[dt][q]; r[dt][q] = z + (st == 0 ? DPPF(z, 0x121) : st == 1 ? DPPF(z, 0x122) : st == 2 ? DPPF(z, 0x124) : DPPF(z, 0x128)); }
          __builtin_amdgcn_sched_barrier(0);
        }
        if (i == 0) {
#pragma unroll
          for (int dt = 0; dt < 8; dt++) *(v4f*)(L + 8192 + pass * 1024 + m * 128 + dt * 16 + 4 * g) = r[dt];
        }
      }
    }
  }
  STAMP(42);
  LBAR();
  if (t < 256) {
    float z = 0.f;
#pragma unroll
    for (int q = 0; q < 8; q++) z += L[8192 + (t >> 7) * 1024 + q * 128 + (t & 127)];
    L[t] = z;
  }
  asm volatile("s_waitcnt vmcnt(0)" ::: "memory");
  LBAR();
  if (which == 0) {
    if (m < 8) {
#pragma unroll
      for (int dt = 0; dt < 8; dt++) D[dt] = (D[dt] - *(const v4f*)(L + dt * 16 + 4 * g) * (1.f / 128.f)) * (LOG2E * 0.08838834764831845f);
    }
  } else if (m < 4) {
    const float* p = L + ((i & 3) == 1 ? 28672 : ((i & 1) ? 128 : 0)) + 32 * m + 4 * g;
    v4f a = *(const v4f*)p, c = *(const v4f*)(p + 16);
    v4u o = {pk2(a[0], a[1]), pk2(a[2], a[3]), pk2(c[0], c[1]), pk2(c[2], c[3])};
    if ((i & 7) == 5) o = v4u{pk2(a[0] - bflo(o[0]), a[1] - bfhi(o[0])), pk2(a[2] - bflo(o[1]), a[3] - bfhi(o[1])), pk2(c[0] - bflo(o[2]), c[1] - bfhi(o[2])), pk2(c[2] - bflo(o[3]), c[3] - bfhi(o[3]))};
    if ((i & 3) == 0) o = v4u{0x3f803f80u, 0x3f803f80u, 0x3f803f80u, 0x3f803f80u};
    __builtin_amdgcn_raw_buffer_store_b128(o, rsw, 65536 + m * 1024 + lane * 16, 0, 16);
  }
  STAMP(43);
  if (m < 8) {
#pragma unroll
    for (int s = 0; s < 4; s++) {
      const v4u o = {pk2(D[2 * s][0], D[2 * s][1]), pk2(D[2 * s][2], D[2 * s][3]), pk2(D[2 * s + 1][0], D[2 * s + 1][1]), pk2(D[2 * s + 1][2], D[2 * s + 1][3])};
      __builtin_amdgcn_raw_buffer_store_b128(o, rsw, which * 32768 + (m * 4 + s) * 1024 + lane * 16, 0, 16);
    }
  }
}

#define TLBS 524288
#define K1P 2
#define K1AUX 16
#define K2SYM "_Z2k2PKtS0_S0_S0_S0_S0_S0_PcPt"
__global__ void __launch_bounds__(640) k1(const int* __restrict__ src, const int* __restrict__ dst, const u16* __restrict__ x,
                                          const u16* __restrict__ dis, const u16* __restrict__ wq, const u16* __restrict__ wk,
                                          const u16* __restrict__ wv, const u16* __restrict__ wo, char* __restrict__ ws, const u16* __restrict__ out) {
#define KID 0
  __shared__ __attribute__((aligned(16))) u32 L[8704];
  const int t = threadIdx.x, b = blockIdx.x;
  const __amdgpu_buffer_rsrc_t rs = __builtin_amdgcn_make_buffer_rsrc(ws, 0, WS_DBG, 0x00020000);
  STAMP_INIT STAMP(0);
  u32 tl = 0;
  if (t >= 576) tl = t < 576 + 58 ? __builtin_amdgcn_raw_buffer_load_b32(rs, (t - 576) * TLBS, 0, 0) : *(const u32*)((const char*)out + (t - 634) * 500000);
  if (b < 128) {
    u32* stage = L; u32* cnt = L + 8000; u32* novf = L + 8256;
    v4u s4 = {0, 0, 0, 0}, d4 = {0, 0, 0, 0};
    if (t < 625) { s4 = *(const v4u*)(src + b * 2500 + t * 4); d4 = *(const v4u*)(dst + b * 2500 + t * 4); }
    u32 pf = 0;
    if (t >= 632) pf = *(const u32*)(((b & 64) ? ((b & 32) ? wo : wv) : ((b & 32) ? wk : wq)) + (b & 31) * 512 + (t - 632) * 64);
    if (t < 257) cnt[t] = 0;
    if (b == 0 && t == 0) *(u32*)(ws + WS_CNT) = 0;
    __syncthreads();
    STAMP(1);
    u32* ovf = (u32*)(ws + WS_OVF1) + b * 2504;
    if (t < 625) {
      u32 r[4], bin[4], en[4];
#pragma unroll
      for (int i = 0; i < 4; i++) {
        u32 d = d4[i];
        bin[i] = (d * 52429u) >> 21;
        r[i] = atomicAdd(&cnt[bin[i]], 1u);
        en[i] = tabidx(s4[i]) | ((d - bin[i] * 40u) << 16);
      }
#pragma unroll
      for (int i = 0; i < 4; i++) {
        if (r[i] < 31) stage[bin[i] * 32 + 1 + r[i]] = en[i];
        else { u32 o = atomicAdd(novf, 1u); ovf[4 + o] = (en[i] & 0xffffu) | (d4[i] << 16); }
      }
    }
    __syncthreads();
    STAMP(2);
    if (t == 0) ovf[0] = *novf;
    asm volatile("" :: "v"(pf));
#pragma unroll
    for (int it = 0; it < 4; it++) {
      int i = t + it * 640;
      if (i < 2000) {
        int bin = i >> 3, q = i & 7;
        u32 c = cnt[bin];
        if ((u32)(q * 4) <= c) {
          v4u v = *(const v4u*)(stage + bin * 32 + q * 4);
          if (q == 0) v[0] = c;
          __builtin_amdgcn_raw_buffer_store_b128(v, rs, WS_SUB + b * 128 + bin * 16384 + q * 16, 0, AUX1);
        }
      }
    }
    STAMP(3);
  } else {
    const int wb = b - 128;
    char* S = (char*)L;
    v4u v[2]; float dv[2]; u32 go[2];
#pragma unroll
    for (int it = 0; it < 2; it++) {
      int i = t + it * 640; int node = i >> 4, cq = i & 15;
      v[it] = *(const v4u*)(x + (size_t)(wb * 80 + node) * 128 + cq * 8);
      dv[it] = bf2f(dis[wb * 80 + node]);
    }
#pragma unroll
    for (int it = 0; it < 2; it++) {
      int i = t + it * 640; int g = i / 40, pc = i - g * 40;
      go[it] = g * GSTRIDE + (pc < 32 ? (pc >> 4) * 32768 + wb * 256 + (pc & 15) * 16 : 65536 + wb * 128 + (pc - 32) * 16);
    }
#pragma unroll
    for (int it = 0; it < 2; it++) {
      int i = t + it * 640; int node = i >> 4, cq = i & 15;
      int s = node >= 40, l = node - s * 40;
      int pos = l < 32 ? (l >> 4) * 32 + s * 16 + (l & 15) : 32 + s * 8 + l;
      v2u a, c;
      a[0] = pk2s(bflo(v[it][0]) * dv[it], bfhi(v[it][0]) * dv[it]);
      a[1] = pk2s(bflo(v[it][1]) * dv[it], bfhi(v[it][1]) * dv[it]);
      c[0] = pk2s(bflo(v[it][2]) * dv[it], bfhi(v[it][2]) * dv[it]);
      c[1] = pk2s(bflo(v[it][3]) * dv[it], bfhi(v[it][3]) * dv[it]);
      *(v2u*)(S + (2 * cq) * 656 + pos * 8) = a;
      *(v2u*)(S + (2 * cq + 1) * 656 + pos * 8) = c;
    }
    __syncthreads();
    STAMP(2);
#pragma unroll
    for (int it = 0; it < 2; it++) {
      int i = t + it * 640; int g = i / 40, pc = i - g * 40;
      v4u o = *(const v4u*)(S + g * 656 + pc * 16);
      __builtin_amdgcn_raw_buffer_store_b128(o, rs, WS_G + go[it], 0, AUX1);
    }
    const v4u pz = {POISON, POISON, POISON, POISON};
#pragma unroll
    for (int it = 0; it < K1P; it++) __builtin_amdgcn_raw_buffer_store_b128(pz, rs, WS_G + go[it & 1], (1 + (it >> 1)) * GBUF, K1AUX);
    __builtin_amdgcn_raw_buffer_store_b128(pz, rs, WS_ELL + wb * 10240 + t * 16, 0, 16);
    if (t < 2) __builtin_amdgcn_raw_buffer_store_b128(pz, rs, t == 0 ? WS_HDR + wb * 16 : (wb < 64 ? WS_FLG + wb * 16 : OOB), 0, 16);
    STAMP(3);
  }
  asm volatile("" :: "v"(tl));
  STAMP_END;
#undef KID
}

#define B_ELL 0
#define B_CB 1280
#define B_CN 1472
#define B_BUSY 2240
#define B_FULL 2432
#define B_ROWM 2480
#define B_NT 2576
#define B_NF 2577
#define B_Z0 1280
#define B_ZN 1304
#define B_Q 2592
#define B_F 20480
#define FID t
#define B_N1 2578
#define B_NM 2579
#define B_NO 2580
#define B_M2 2581
#define B_BASE 2582
template <class F, class F2>
__device__ __forceinline__ void build(const int slot, char* __restrict__ ws, u32* L, F&& early, F2&& early2 WST) {
  const int t = threadIdx.x, lane = t & 63, w = __builtin_amdgcn_readfirstlane(t >> 6);
  const __amdgpu_buffer_rsrc_t rs = __builtin_amdgcn_make_buffer_rsrc(ws, 0, WS_DBG, 0x00020000);
  v4u v; u32 n1;
  {
    const v4u srd = {(u32)(size_t)ws, (u32)((size_t)ws >> 32) & 0xffffu, WS_DBG, 0x00020000u};
    asm volatile("buffer_load_dwordx4 %0, %2, %4, %5 offen\n\tbuffer_load_dword %1, %3, %4, 0 offen"
                 : "=&v"(v), "=&v"(n1) : "v"(t * 16), "v"(t < 128 ? WS_OVF1 + t * 10016u : OOB), "s"(srd), "s"(WS_SUB + slot * 16384) : "memory");
  }
  if (t < 320) { const v4u z = {POISON, POISON, POISON, POISON}; *(v4u*)(L + B_ELL + t * 4) = z; }
  else if (t < 320 + B_ZN / 4) { const v4u z = {0, 0, 0, 0}; *(v4u*)(L + B_Z0 + (t - 320) * 4) = z; }
  LBAR();
  early();
  asm volatile("s_waitcnt vmcnt(5)" : "+v"(v), "+v"(n1) :: "memory");
  STAMP(40);
  {
    const u32 q = t & 7;
    u32 c = DPPU(v[0], 0x00);
    c = (u32)__builtin_amdgcn_update_dpp((int)c, (int)c, 0x114, 0xF, 0xA, false);
    c = min(c, 31u);
    u32 inc = c + (u32)__builtin_amdgcn_update_dpp(0, (int)c, 0x118, 0xF, 0xC, false);
    inc += (u32)__builtin_amdgcn_update_dpp(0, (int)inc, 0x142, 0xA, 0xF, false);
    inc += (u32)__builtin_amdgcn_update_dpp(0, (int)inc, 0x143, 0xC, 0xF, false);
    u32 base = 0;
    if (lane == 63) base = atomicAdd(&L[B_NT], inc);
    base = __builtin_amdgcn_readlane(base, 63);
    const u32 pos = B_Q + base + inc - c + 4 * q - 1;
#pragma unroll
    for (int i = 0; i < 4; i++) {
      const u32 id = 4 * q + i;
      L[(id - 1u < c) ? pos + i : B_Q + 4096 + lane] = v[i];
    }
  }
  if (n1) atomicAdd(&L[B_N1], n1);
  early2();
  LBAR();
  STAMP(41);
  auto place = [&](u32 e, u32 r, u32 c) {
    const u32 loc = e >> 16, hp = loc >> 3;
    const u32 j = hp == 4 ? 24 + (r >> 1) : (r < 12 ? (hp >> 1) * 12 + r : 20 + (hp >> 1) * 4 + r);
    const u32 half = (hp == 4 ? r : hp) & 1;
    const u32 ln = half * 32 + (loc & 4) * 4 + c * 4 + (loc & 3);
    ((u16*)(L + B_ELL))[(j >> 3) * 512 + ln * 8 + (j & 7)] = (u16)e;
    atomicOr(&L[B_ROWM + half * 40 + j], 1u << ((e + 16) & 31));
  };
  const u32 tot = L[B_NT];
  for (u32 b0 = w * 128; b0 < tot; b0 += 2048) {
    u32 e[2], hb[2], rk[2], r[2], c[2];
#pragma unroll
    for (int i = 0; i < 2; i++) { const u32 id = b0 + i * 64 + lane; e[i] = L[B_Q + id]; if (id >= tot) e[i] = ((40 + (lane & 7)) << 16) | (lane & 31); }
#pragma unroll
    for (int i = 0; i < 2; i++) { hb[i] = (e[i] >> 19) * 32 + (e[i] & 31); rk[i] = atomicAdd(&L[B_CB + hb[i]], 1u); }
#pragma unroll
    for (int i = 0; i < 2; i++) {
      r[i] = e[i] >= (32u << 16) ? (rk[i] + (e[i] >> 1)) & 15 : (rk[i] < 12 ? rk[i] : rk[i] - 12);
      c[i] = atomicAdd(&L[rk[i] < 24 ? B_CN + (e[i] >> 16) * 16 + r[i] : B_CN + 640 + lane], 1u);
    }
#pragma unroll
    for (int i = 0; i < 2; i++) {
      if (e[i] < (40u << 16)) {
        if (rk[i] < 24 && c[i] < 4) {
          place(e[i], r[i], c[i]);
          atomicOr(&L[B_BUSY + hb[i]], 1u << r[i]);
          if (c[i] == 3) atomicOr(&L[B_FULL + (e[i] >> 16)], 1u << r[i]);
        } else L[B_F + atomicAdd(&L[B_NF], 1u)] = e[i];
      }
    }
  }
  LBAR();
  STAMP(42);
  const u32 nf = L[B_NF];
  for (u32 id = FID; id < nf; id += 1024) {
    const u32 e = L[B_F + id], loc = e >> 16, hb = (e >> 19) * 32 + (e & 31);
    u32 rho = (id * 5 + (id >> 2)) & 15, tried = 0;
    const u32 pm = loc >= 32 ? 0xFFFFu : 0xFFFu;
    for (int round = 0;; round++) {
      const u32 bs = __hip_atomic_load(&L[B_BUSY + hb], __ATOMIC_RELAXED, __HIP_MEMORY_SCOPE_WORKGROUP);
      const u32 nfl = ~(__hip_atomic_load(&L[B_FULL + loc], __ATOMIC_RELAXED, __HIP_MEMORY_SCOPE_WORKGROUP) | tried);
      u32 cand = nfl & ~bs & pm;
      const bool strict = cand != 0 && round < 2;
      if (!cand) cand = nfl & pm;
      if (!cand) cand = nfl & ~bs & 0xF000;
      if (!cand) cand = nfl & 0xF000;
      if (!cand) { L[B_Q + atomicAdd(&L[B_NO], 1u)] = (e & 0xffffu) | ((slot * 40 + loc) << 16); break; }
      const u32 r = (rho + __builtin_ctz((cand | (cand << 16)) >> rho)) & 15;
      const u32 ob = atomicOr(&L[B_BUSY + hb], 1u << r);
      const u32 c = atomicAdd(&L[B_CN + loc * 16 + r], 1u);
      if (c == 3) atomicOr(&L[B_FULL + loc], 1u << r);
      if (c < 4 && !(strict && ((ob >> r) & 1))) { place(e, r, c); break; }
      if (c >= 3) tried |= 1u << r;
      rho = (rho + 7) & 15;
    }
  }
  auto scan = [&](auto&& fn) {
    for (int j = 0; j < 128; j++) {
      const u32* o1 = (const u32*)(ws + WS_OVF1) + j * 2504;
      const u32 n = o1[0];
      for (u32 i = t; i < n; i += 1024) { const u32 en = o1[4 + i]; if ((en >> 16) - slot * 40u < 40u) fn(en); }
    }
  };
  const bool sp = L[B_N1] != 0;
  if (sp) scan([&](u32) { atomicAdd(&L[B_NM], 1u); });
  asm volatile("s_waitcnt vmcnt(0)" ::: "memory");
  LBAR();
  STAMP(43);
  const u32 no = L[B_NO], nt = no + L[B_NM];
  u32 base = 0;
  if (nt) {
    if (t == 0) L[B_BASE] = atomicAdd((u32*)(ws + WS_CNT), nt);
    LBAR();
    base = L[B_BASE];
    for (u32 i = t; i < no; i += 1024) __builtin_amdgcn_raw_buffer_store_b32(L[B_Q + i], rs, WS_OVF2 + (base + i) * 4, 0, 16);
    if (sp) scan([&](u32 en) { __builtin_amdgcn_raw_buffer_store_b32(en, rs, WS_OVF2 + (base + no + atomicAdd(&L[B_M2], 1u)) * 4, 0, 16); });
    asm volatile("s_waitcnt vmcnt(0)" ::: "memory");
    LBAR();
  }
  if (t < 320) {
    v4u o = *(const v4u*)(L + B_ELL + t * 4);
    const u32* rm = L + B_ROWM + (lane >> 5) * 40 + w * 8;
    const v4u m0 = *(const v4u*)rm, m1 = *(const v4u*)(rm + 4);
#pragma unroll
    for (int i = 0; i < 4; i++) {
      const u32 a = i < 2 ? m0[2 * i] : m1[2 * i - 4], bb = i < 2 ? m0[2 * i + 1] : m1[2 * i - 3];
      u32 fa, fb;
      asm("v_ffbl_b32 %0, %1" : "=v"(fa) : "v"(~a)); asm("v_ffbl_b32 %0, %1" : "=v"(fb) : "v"(~bb));
      v2h d = {(u16)(fa + ZROW), (u16)(fb + ZROW)};
      const u32 oi = o[i];
      o[i] = __builtin_bit_cast(u32, __builtin_elementwise_min(__builtin_bit_cast(v2h, oi), d));
    }
    __builtin_amdgcn_raw_buffer_store_b128(o, rs, WS_ELL + slot * 5120 + t * 16, 0, 16);
  }
  STAMP(5);
  if (t == 320) __builtin_amdgcn_raw_buffer_store_b64(v2u{base, nt}, rs, WS_HDR + slot * 8, 0, 16);
}

#define L_WM 40960u
#define L_FLAG 160000u
#define L_WR 32768u
#define L_WW 81920u
__device__ __forceinline__ u32 chunk(u32 id) { return id * 2048u + (id >= 20u ? 40960u : 0u); }
__device__ __forceinline__ v4u ld128(u32 a) { return *(const LAS v4u*)(size_t)a; }
__device__ __forceinline__ v2u ld64(u32 a) { return *(const LAS v2u*)(size_t)a; }
__device__ __forceinline__ float bperm(u32 a, float v) { return __uint_as_float((u32)__builtin_amdgcn_ds_bpermute((int)a, (int)__float_as_uint(v))); }
__device__ __forceinline__ float dot2(u32 a, u32 b, float c) { return __builtin_amdgcn_fdot2_f32_bf16(__builtin_bit_cast(v2bf, a), __builtin_bit_cast(v2bf, b), c, false); }
#define RD 2
#define SB() __builtin_amdgcn_sched_barrier(0)
template <int I, int E, class F> __device__ __forceinline__ void sfor(F&& fn) {
  if constexpr (I < E) { fn(std::integral_constant<int, I>{}); sfor<I + 1, E>(fn); }
}
#define fmx(a, b) __builtin_amdgcn_fmed3f((a), (b), finf)
#define PS1 24
#define PS2 48
#define FO(mm) ((mm) == 0 ? 39 : 38 + 4 * (mm) - ((mm) == 7))
#define OPQ(x) asm volatile("" : "+v"(x))
#define OPQ2(x, y) asm volatile("" : "+v"(x), "+v"(y))
__device__ __forceinline__ float sel(bool c, float a, float b) { asm("" : "+v"(a)); return c ? a : b; }
__device__ __forceinline__ u32 pk2o(float a, float b) { asm("" : "+v"(a)); asm("" : "+v"(b)); return pk2(a, b); }
struct TfPre { u32 aT, aR, so, bp, bq; };
__device__ __forceinline__ TfPre tfpre(const u32 j0, const u32 slot, const u32 lane) {
  const u32 g = lane >> 4, n = lane & 15, k = n >> 1, nd = n & 1, u = j0 >> 2, kr = 2 * g + ((n >> 2) & 1);
  TfPre p;
  p.aT = chunk(u * 8 + k) + (g ^ ((k >> 2) << 1)) * 64 + ((j0 ^ k) & 3) * 16 + nd * 8;
  p.aR = chunk(u * 8 + kr) + ((n & 3) ^ ((kr >> 2) << 1)) * 64 + ((j0 ^ kr) & 3) * 16 + (n >> 3) * 8;
  p.so = (slot * 40 + 2 * j0) * 256;
  p.bp = (20 * g + nd) * 4; p.bq = (16 * (n >> 2) + n) * 4;
  OPQ2(p.aT, p.aR); OPQ2(p.bp, p.bq);
  return p;
}
template <int NT, int PQ>
__device__ __forceinline__ void tf(const TfPre& P, const u32 lane, const float dv0, const float dv1, u16* __restrict__ out) {
  const u32 g = lane >> 4, n = lane & 15, k = n >> 1, nd = n & 1;
  const u32 aT = P.aT, bp = P.bp, bq = P.bq;
  u32 aW = lane * 16 + L_WM;
  float finf = __builtin_inff(); asm("" : "+s"(finf));
  v4u TF[NT][4], W[RD];
#pragma unroll
  for (int s = 0; s < 4; s++) {
#pragma unroll
    for (int t = 0; t < NT; t++) {
      const v2u a = ld64((aT ^ (16 * t)) + 512 * s), b = ld64((aT ^ (16 * t)) + 512 * s + 256);
      TF[t][s] = v4u{a[0], a[1], b[0], b[1]};
    }
    if (s < RD) W[s] = ld128(aW + L_WR + s * 1024);
  }
  SB();
  u32 aR[2] = {P.aR, P.aR ^ 16};
  OPQ(aR[1]);
  const float dve[2] = {dv0, dv1};
  float rho[NT], rb[NT][2], c1[NT][2], c3[NT][2], v0[NT], v1[NT], mx[2], zs[2], l1[2], l3[2], q2[2][2];
  v4f S[NT], LN[NT], GR[NT], U[NT][8], y[NT][8], o[NT][8];
  v4u ub[NT], B[NT], YQ[NT][4]; v2u Y[NT][8], tr[NT][8]; u32 dg0[NT], dg1[NT];
  const v4f Z4 = {0, 0, 0, 0};
  float dr; u32 wfl;
  SB();
  sfor<-4, 68>([&](auto Fc) __attribute__((always_inline)) {
    constexpr int f = decltype(Fc)::value, m = (f >> 2) & 7, s = f & 3, fn = f + RD;
    if constexpr (f == -4) __builtin_amdgcn_s_setprio(PQ & 3);
    if constexpr (f == PS1) __builtin_amdgcn_s_setprio((PQ >> 4) & 3);
    if constexpr (f == PS2) __builtin_amdgcn_s_setprio((PQ >> 8) & 3);
    if constexpr (f >= 10 && f <= 34 && (f & 7) == 2) {
#pragma unroll
      for (int t = 0; t < NT; t++) S[t] = MFMA(TF[t][(f - 10) >> 3], ub[t], (f > 10 ? S[t] : Z4));
      SB();
    }
#pragma unroll
    for (int mm = 0; mm < 8; mm++) {
      if (f == FO(mm)) {
#pragma unroll
        for (int t = 0; t < NT; t++) o[t][mm] = MFMA((v4u{tr[t][mm][0], tr[t][mm][1], Y[t][mm][0], Y[t][mm][1]}), B[t], Z4);
        SB();
      }
    }
    if constexpr (f < 64) {
      const v4u wf = W[(f + 4) % RD];
#pragma unroll
      for (int t = 0; t < NT; t++) {
        if constexpr (f < 0) { GR[t] = MFMA(TF[t][s], TF[t][s], (s ? GR[t] : Z4)); LN[t] = MFMA(wf, TF[t][s], (s ? LN[t] : Z4)); }
        else if constexpr (f < 32) U[t][m] = MFMA(wf, TF[t][s], (s ? U[t][m] : Z4));
        else y[t][m] = MFMA(TF[t][s], wf, (s ? y[t][m] : Z4));
      }
      if constexpr (fn == 30) wfl = *(volatile LAS u32*)(size_t)L_FLAG;
      if constexpr (fn == 32) {
        u32 sa, sc;
        asm volatile("s_mov_b32 %2, 0\n"
                     "L_wf%=:\n\tv_readfirstlane_b32 %1, %0\n\ts_add_u32 %2, %2, 1\n\ts_lshr_b32 %1, %1, 2\n\ts_lshr_b32 vcc_lo, %2, 17\n\ts_or_b32 %1, %1, vcc_lo\n\ts_cbranch_scc1 L_wd%=\n"
                     "\tv_mov_b32 %0, %3\n\tds_read_b32 %0, %0\n\ts_waitcnt lgkmcnt(0)\n\ts_branch L_wf%=\n"
                     "L_wd%=:" : "+v"(wfl), "=&s"(sa), "=&s"(sc) : "n"(L_FLAG) : "vcc", "scc", "memory");
        aW += L_WW; OPQ(aW);
      }
      if constexpr (fn < 64)
      W[(f + 4) % RD] = ld128(aW + (fn < 0 ? L_WR + (fn + 4) * 1024 : (fn & 31) * 1024));
    }
#pragma unroll
    for (int t = 0; t < NT; t++) {
      if (f == 1 + 2 * t) {
        const float qa = sel(nd, GR[t][1], GR[t][0]), qb = sel(nd, GR[t][3], GR[t][2]), q = (n & 2) ? qb : qa;
        const float a = LN[t][0] * (1.f / 128.f);
        rho[t] = fmaxf(q * (1.f / 128.f) - a * a, 0.f) + (k == 0 ? 1e-5f : 1e-5f * dve[t] * dve[t]);
        OPQ(rho[t]);
      }
      if (f == 2 + 2 * t) {
        rho[t] = __builtin_amdgcn_rsqf(rho[t]); rho[t] = k == 0 ? rho[t] : __builtin_copysignf(rho[t], dve[t]);
        const float dd = g < 2 ? (k == 0 ? dve[t] : 1.f) : 0.f;
        l1[t] = 0.5f * dd * LN[t][0]; l3[t] = dd * LN[t][1];
        OPQ2(l1[t], l3[t]);
      }
      if (f == 6 + t) {
        rb[t][0] = bperm(bp, rho[t]); rb[t][1] = bperm(bp + 8, rho[t]); rho[t] = bperm(bq, rho[t]);
      }
      if (f == 8 + t) {
        const u32 d1 = k == 0 ? __float_as_uint(dve[t]) : 0x3f800000u, d2 = (k >> 1) == g ? d1 : 0u, dg = d2 >> ((n & 2) ? 0 : 16);
        dg0[t] = nd ? 0u : dg; dg1[t] = nd ? dg : 0u; OPQ2(dg0[t], dg1[t]);
      }
      if (f == 41) { c1[t][0] = bperm(bp, LN[t][2]); c1[t][1] = bperm(bp + 8, LN[t][2]); c3[t][0] = bperm(bp, LN[t][3]); c3[t][1] = bperm(bp + 8, LN[t][3]); }
      if (f == 12) { v0[t] = rb[t][0] * rho[t]; v1[t] = rb[t][1] * rho[t]; OPQ2(v0[t], v1[t]); rb[t][0] *= dve[t]; rb[t][1] *= dve[t]; OPQ2(rb[t][0], rb[t][1]); }
    }
    if (f == 13) { dr = NT == 2 ? sel(g & 1, dv1, dv0) : dv0; OPQ(dr); }
    if constexpr (f >= 5 && f <= 33 && s == 1) {
      constexpr int mm = (f - 5) >> 2;
#pragma unroll
      for (int t = 0; t < NT; t++) { ub[t][(mm & 1) * 2] = pk2(U[t][mm][0], U[t][mm][1]); ub[t][(mm & 1) * 2 + 1] = pk2(U[t][mm][2], U[t][mm][3]); }
    }
    if (f == 35) {
#pragma unroll
      for (int t = 0; t < NT; t++) { v0[t] *= sel(nd, S[t][1], S[t][0]); v1[t] *= sel(nd, S[t][3], S[t][2]); OPQ2(v0[t], v1[t]); mx[t] = fmx(v0[t], v1[t]); }
      if (NT == 1) mx[1] = mx[0];
      sw16(mx[0], mx[1]); mx[0] = fmx(mx[0], mx[1]);
    }
    if (f == 36) { mx[1] = mx[0]; sw32(mx[0], mx[1]); mx[0] = fmx(mx[0], mx[1]); mx[1] = mx[0]; if (NT == 2) sw16(mx[0], mx[1]); }
    if (f == 36) {
#pragma unroll
      for (int t = 0; t < NT; t++) { v0[t] = __builtin_amdgcn_exp2f(v0[t] - mx[t]); v1[t] = __builtin_amdgcn_exp2f(v1[t] - mx[t]); zs[t] = v0[t] + v1[t]; }
      if (NT == 1) zs[1] = zs[0];
    }
    if (f == 37) { sw16(zs[0], zs[1]); zs[0] += zs[1]; zs[1] = zs[0]; sw32(zs[0], zs[1]); zs[0] = __builtin_amdgcn_rcpf(zs[0] + zs[1]); zs[1] = zs[0]; if (NT == 2) sw16(zs[0], zs[1]); }
    if (f == 38) {
#pragma unroll
      for (int t = 0; t < NT; t++) {
        v0[t] *= rb[t][0]; v1[t] *= rb[t][1]; OPQ(v0[t]); v0[t] *= zs[t]; v1[t] *= zs[t];
        const u32 p = pk2o(v0[t], v1[t]);
        B[t] = v4u{dg0[t], dg1[t], nd ? 0u : p, nd ? p : 0u};
      }
    }
    if (f == 44) {
#pragma unroll
      for (int t = 0; t < NT; t++) {
        l1[t] += v0[t] * c1[t][0]; l3[t] += v0[t] * c3[t][0]; OPQ2(l1[t], l3[t]); l1[t] += v1[t] * c1[t][1]; l3[t] += v1[t] * c3[t][1];
        OPQ2(l1[t], l3[t]);
      }
    }
    if (f == 48) {
#pragma unroll
      for (int t = 0; t < NT; t++) { sw32(l1[t], l3[t]); l1[t] += l3[t]; }
      if (NT == 1) l1[1] = l1[0];
      sw16(l1[0], l1[1]); l1[0] += l1[1];
    }
    if (f == 52) { l3[0] = l1[0]; sw32(l1[0], l3[0]); l1[0] *= 1.f / 128.f; OPQ2(l1[0], l3[0]); }
#pragma unroll
    for (int mm = 0; mm < 8; mm++) {
      const int fo = FO(mm);
      if (f == fo - 3) {
#pragma unroll
        for (int t = 0; t < NT; t++) tr[t][mm] = __builtin_bit_cast(v2u, rd(aR[t] + 256 * mm));
      }
      if (f == 37 + 4 * mm - (mm == 7)) {
#pragma unroll
        for (int t = 0; t < NT; t++) {
          Y[t][mm] = v2u{pk2(y[t][mm][0], y[t][mm][2]), pk2(y[t][mm][1], y[t][mm][3])};
          u32 ya = Y[t][mm][0], yb = Y[t][mm][1];
          asm volatile("v_mov_b32 %0, %2\n\tv_mov_b32 %1, %3" : "=&v"(YQ[t][mm >> 1][2 * (mm & 1)]), "=v"(YQ[t][mm >> 1][2 * (mm & 1) + 1]) : "v"(ya), "v"(yb));
        }
      }
#pragma unroll
      for (int h = 0; h < 2; h++) {
        if (f == fo + 1 + h) {
#pragma unroll
          for (int t = 0; t < NT; t++) {
            const float a = o[t][mm][2 * h], c = o[t][mm][2 * h + 1];
            if (mm == 0 && h == 0) { q2[t][0] = a * a; q2[t][1] = c * c; } else { q2[t][0] += a * a; q2[t][1] += c * c; }
            OPQ2(q2[t][0], q2[t][1]);
          }
        }
      }
    }
    SB();
  });
#pragma unroll
  for (int m = 0; m < 8; m++)
#pragma unroll
    for (int t = 0; t < NT; t++) tr[t][m] = __builtin_bit_cast(v2u, rd(aR[t] + 256 * m));
  u32 ln2 = lane; OPQ(ln2);
  const u32 oo = (ln2 & 1) * 256 + (ln2 & 14) * 16 + (ln2 >> 4) * 8;
  float al;
  {
    float qa = q2[0][0] + q2[0][1], qb = q2[NT - 1][0] + q2[NT - 1][1];
    sw16(qa, qb); qa += qb; qb = qa; sw32(qa, qb);
    const float var = fmaxf((qa + qb) * (1.f / 128.f) - l1[0] * l1[0], 0.f);
    const float lg = l3[0] * __builtin_copysignf(__builtin_amdgcn_rsqf(var + 1e-5f * dr * dr), dr);
    float mm = fmx(lg, DPPF(lg, 0x122)); mm = fmx(mm, DPPF(mm, 0x124)); mm = fmx(mm, DPPF(mm, 0x128));
    const float e = __builtin_amdgcn_exp2f(lg - mm);
    float z = e + DPPF(e, 0x122); z += DPPF(z, 0x124); z += DPPF(z, 0x128);
    al = e * __builtin_amdgcn_rcpf(z);
  }
  const float ald = al * __builtin_amdgcn_rcpf(dr), als = k == 0 ? al : ald;
  float aa[2] = {ald, ald}; if (NT == 2) sw16(aa[0], aa[1]);
  const u32 br = (4 * g + nd) * 4;
  v4f R[NT][4];
#pragma unroll
  for (int t = 0; t < NT; t++) {
    const float a0 = bperm(br + 64 * t, als), a1 = bperm(br + 64 * t + 8, als);
    const u32 p = B[t][2] | B[t][3];
    float b0 = bflo(p) * aa[t], b1 = bfhi(p) * aa[t];
    OPQ(b0);
    b0 += DPPF(b0, 0x122); b1 += DPPF(b1, 0x122); b0 += DPPF(b0, 0x124); b1 += DPPF(b1, 0x124); b0 += DPPF(b0, 0x128); b1 += DPPF(b1, 0x128);
    const u32 xa = pk2o(a0, a1), xb = pk2o(b0, b1), r = n & 3;
    const v4u ga = {r == 0 ? xa : 0u, r == 1 ? xa : 0u, r == 2 ? xa : 0u, r == 3 ? xa : 0u};
    const v4u gb = {r == 0 ? xb : 0u, r == 1 ? xb : 0u, r == 2 ? xb : 0u, r == 3 ? xb : 0u};
#pragma unroll
    for (int c = 0; c < 4; c++) R[t][c] = MFMA((v4u{tr[t][2 * c][0], tr[t][2 * c][1], tr[t][2 * c + 1][0], tr[t][2 * c + 1][1]}), ga, Z4);
#pragma unroll
    for (int c = 0; c < 4; c++) R[t][c] = MFMA(YQ[t][c], gb, R[t][c]);
  }
#pragma unroll
  for (int t = 0; t < NT; t++) {
    u32 r0 = pk2(R[t][0][0], R[t][0][1]), r1 = pk2(R[t][0][2], R[t][0][3]);
#pragma unroll
    for (int c = 1; c < 4; c++) {
      u32 s0 = pk2(R[t][c][0], R[t][c][1]), s1 = pk2(R[t][c][2], R[t][c][3]);
      OPQ2(s0, s1);
      r0 = (n >> 2) == c ? s0 : r0; r1 = (n >> 2) == c ? s1 : r1;
    }
    __builtin_amdgcn_raw_buffer_store_b64(v2u{r0, r1}, __builtin_amdgcn_make_buffer_rsrc(out, 0, NN * 256, 0x00020000), oo + 512 * t, P.so, 0);
  }
}
#define P3A 0x123
#define P3B 0x123
#define P3C 0x123

__device__ __forceinline__ v16bf rd4(u32 a0, u32 a1, u32 a2, u32 a3) {
  const v4bf a = rd(a0), b = rd(a1), c = rd(a2), d = rd(a3);
  return __builtin_shufflevector(__builtin_shufflevector(a, b, 0, 1, 2, 3, 4, 5, 6, 7), __builtin_shufflevector(c, d, 0, 1, 2, 3, 4, 5, 6, 7),
                                 0, 1, 2, 3, 4, 5, 6, 7, 8, 9, 10, 11, 12, 13, 14, 15);
}
#define DMA(voff, soff, lbase) asm volatile("s_mov_b32 m0, %2\n\ts_nop 0\n\tbuffer_load_dwordx4 %0, %3, %1 offen sc1 lds" :: "v"(voff), "s"(soff), "s"(__builtin_amdgcn_readfirstlane(lbase)), "s"(srd) : "memory")
#define PFM 1
#define PR1 0
#define PR2 2
#define PR3 4
#define ELLAUX 16
#define PF0 tokdma(w);
#define PF1
#define PF2 tokdma(w + 16);
#define PF3 tokdma(w + 32);
#define PF4 if (w < 12) tokdma(w + 48);
#define NPRE 1
#define KMAX 7
#define LASTP 7
#define D23POS 0
#define D4POS 2
#define T2PRE
__global__ void __launch_bounds__(1024) k2(const u16* __restrict__ dis, const u16* __restrict__ wq, const u16* __restrict__ wk, const u16* __restrict__ wv,
                                           const u16* __restrict__ wo, const u16* __restrict__ wr, const u16* __restrict__ x, char* __restrict__ ws, u16* __restrict__ out) {
#define KID 2
  __shared__ __attribute__((aligned(16))) char T[163840];
  const int t = threadIdx.x, lane = t & 63, w = __builtin_amdgcn_readfirstlane(t >> 6);
  const int b = blockIdx.x, gp = (b & 7) * 2 + (b >> 7), p = (b >> 3) & 15;
  const int slot = p * 16 + w;
  const bool act = slot < 250;
  asm volatile("" :: "v"((u32)(size_t)(LAS char*)T));
  STAMP_INIT STAMP(0);
  const __amdgpu_buffer_rsrc_t rs = __builtin_amdgcn_make_buffer_rsrc(ws + WS_G, 0, 8u * GBUF, 0x00020000);
  const v4u srd = {(u32)(size_t)ws, (u32)((size_t)ws >> 32) & 0xffffu, WS_DBG, 0x00020000u};
  const u32 sgA = gp * 2 * GSTRIDE;
  const u32 vla = w * 1024 + lane * 16;
  const u32 vlc = (w == 15 && lane >= 40) ? OOB : vla;
  const u32 lb0 = w * 1024, lb2 = w * 1024 + 32768 + (w >= 8 ? 40960 : 0);
  const int bs = p * 16 + gp;
  const u32 vpz = w < 10 ? (w >= 5 ? GSTRIDE : 0u) + ((w % 5) >> 1) * 32768 + p * (w % 5 < 4 ? 2048 : 1024) + ((w % 5) & 1) * 1024 + lane * 16
                         : (w == 12 ? (lane < 17 ? WS_W + ((b >> 3) * 17 + lane) * 128 : OOB) : (lane & 31) * GSTRIDE + (w - 13) * 32768 + min(bs, 249) * (w < 15 ? 128 : 64));
  auto touch = [&](const u32 so) __attribute__((always_inline)) {
    asm volatile("s_mov_b32 m0, 0x27f80\n\ts_mov_b32 exec_hi, 0\n\tbuffer_load_dword %0, %1, %2 offen sc1 lds\n\ts_mov_b32 exec_hi, -1" :: "v"(vpz), "s"(srd), "s"(so) : "memory");
  };
  auto early = [&]() __attribute__((always_inline)) {
    DMA(vla, sgA, lb0); DMA(vla, sgA + 16384, lb0 + 16384); DMA(vla, sgA + 32768, lb2); DMA(vla, sgA + 49152, lb0 + 90112); DMA(vlc, sgA + 65536, lb0 + 106496);
  };
  auto early2 = [&]() __attribute__((always_inline)) {
    if (w < 10) {
      const v4u pz = {POISON, POISON, POISON, POISON};
      DMA(vpz, sgA + GBUF, 156672);
      asm volatile("buffer_store_dwordx4 %0, %1, %2, %3 offen sc1" :: "v"(pz), "v"(vpz), "s"(srd), "s"(sgA + 2 * GBUF) : "memory");
    }
  };
  {
    if (bs < 250) build(bs, ws, (u32*)(T + 40960), early, early2 WSA);
    else {
      if (bs < 252) wprep(bs - 250, wq, wk, wv, wo, wr, ws, (float*)(T + 40960), [&]() { early(); early2(); } WSA);
      else { early(); early2(); asm volatile("s_waitcnt vmcnt(0)" ::: "memory"); }
    }
  }
  if (t < 32) { const v4u z = {0, 0, 0, 0}; *(LAS v4u*)(size_t)(ZROW * 8 + (t >> 4) * 40960 + (t & 15) * 16) = z; }
  LBAR();
  STAMP(44);
  v4u e[5]; u16 dvr[3];
  const int sl = act ? slot : 0;
  dvr[0] = dis[sl * 40 + (lane >> 2)]; dvr[1] = dis[sl * 40 + 16 + (lane >> 2)]; dvr[2] = dis[sl * 40 + 32 + ((lane & 31) >> 2)];
  const __amdgpu_buffer_rsrc_t rw = __builtin_amdgcn_make_buffer_rsrc(ws + WS_ELL, 0, WS_W - WS_ELL, 0x00020000);
  v2u hd = __builtin_amdgcn_raw_buffer_load_b64(rw, 0, WS_HDR - WS_ELL + sl * 8, 16);
#pragma unroll
  for (int i = 0; i < 5; i++) e[i] = __builtin_amdgcn_raw_buffer_load_b128(rw, lane * 16 + i * 1024, sl * 5120, ELLAUX);
  for (u32 spins = 0;; spins++) {
    u32 m = max(hd[0], hd[1]);
#pragma unroll
    for (int i = 0; i < 5; i++) m = max(max(m, max(e[i][0], e[i][1])), max(e[i][2], e[i][3]));
    if (__builtin_amdgcn_ballot_w64(m == POISON) == 0 || spins >= MAXSPIN) break;
    __builtin_amdgcn_s_sleep(2);
    asm volatile("" ::: "memory");
    hd = __builtin_amdgcn_raw_buffer_load_b64(rw, 0, WS_HDR - WS_ELL + sl * 8, 16);
#pragma unroll
    for (int i = 0; i < 5; i++) e[i] = __builtin_amdgcn_raw_buffer_load_b128(rw, lane * 16 + i * 1024, sl * 5120, 16);
  }
  const u32 novf = act ? min((u32)__builtin_amdgcn_readfirstlane(hd[1]), (u32)EE) : 0u;
  const u32 ovb = WS_OVF2 - WS_ELL + min((u32)__builtin_amdgcn_readfirstlane(hd[0]), (u32)EE) * 4;
  STAMP(45);
  const u32 vp01 = act ? slot * 128 + (lane >> 1) * 4 : OOB;
  const u32 vp2 = act ? 65536 + slot * 64 + (lane >> 1) * 4 : OOB;
  v8bf sel1, sel2; const int seli = (lane & 1) ? 0xEEEEEEEE : 0x44444444;
  {
    const u32 on = (lane & 2) ? 0u : 0x3f803f80u, lo = (lane & 4) ? 0u : on, hi = (lane & 4) ? on : 0u;
    const bool m1 = ((lane >> 4) & 1) == ((lane >> 3) & 1);
    const v4u a2 = {lo, lo, hi, hi}, a1 = {m1 ? lo : 0u, m1 ? lo : 0u, m1 ? hi : 0u, m1 ? hi : 0u};
    sel1 = __builtin_bit_cast(v8bf, a1); sel2 = __builtin_bit_cast(v8bf, a2);
  }
  u32 vchk;
  {
    const u32 i = (lane >> 3) & 3, ln = (w == 15 && (i & 1)) ? (lane & 1) : (lane & 7);
    const u32 hl = (w == 15 && (lane & 15) >= 10) ? (lane & 7) : (lane & 15);
    vchk = lane < 32 ? (i == 2 ? lb2 : lb0 + (i == 1 ? 16384 : (i == 3 ? 90112 : 0))) + ln * 128 + lane * 4 : lb0 + 106496 + hl * 64 + (lane & 15) * 4;
  }
  u32 ad[32];
#pragma unroll
  for (int i = 0; i < 16; i++) { const u32 q = e[i >> 2][i & 3]; ad[2 * i] = (q & 0xffffu) * 8; ad[2 * i + 1] = (q >> 16) * 8; }
  u32 ex[4] = {e[4][0], e[4][1], e[4][2], e[4][3]};
  bool xm;
  {
    const u32 m0 = min(min(ex[0] & 0xffffu, ex[0] >> 16), min(ex[1] & 0xffffu, ex[1] >> 16));
    const u32 m1 = min(min(ex[2] & 0xffffu, ex[2] >> 16), min(ex[3] & 0xffffu, ex[3] >> 16));
    xm = __builtin_amdgcn_ballot_w64(min(m0, m1) < ZROW) != 0;
  }
  float dv2[3];
#pragma unroll
  for (int i = 0; i < 3; i++) { const float d = bf2f(dvr[i]); dv2[i] = d * d; }
  STAMP(1);

  auto slow = [&](const u32 tofs, const u32 n0, const u32 nn) __attribute__((always_inline)) -> float {
    float a = 0.f;
    for (u32 base = 0; base < novf; base += 64) {
      const u32 en = base + lane < novf ? __builtin_amdgcn_raw_buffer_load_b32(rw, (base + lane) * 4, ovb, 16) : POISON;
      u64 m = __builtin_amdgcn_ballot_w64((en >> 16) - n0 < nn);
      while (m) {
        const int l = __builtin_ctzll(m); m &= m - 1;
        const u32 es = __builtin_amdgcn_readlane(en, l);
        const float val = bf2f(*(LAS u16*)(size_t)((es & 0xffffu) * 8 + tofs + (lane & 3) * 2));
        a += ((es >> 16) - n0 == (u32)(lane >> 2)) ? val : 0.f;
      }
    }
    return a;
  };
  auto grow = [&](const u32 la, const u32 scur) __attribute__((always_inline)) -> v2u {
    v2u v = {0, 0};
    if (la < ZROW * 8) {
      u32 spins = 0;
      do {
        v = __builtin_amdgcn_raw_buffer_load_b64(rs, la >= 81920 ? la - 40960 : la, scur, 16);
        asm volatile("" ::: "memory");
      } while (max(v[0], v[1]) == POISON && ++spins < MAXSPIN);
    }
    return v;
  };
  auto fallback = [&](const int bt, const u32 scur) __attribute__((always_inline)) -> float {
    float s0 = 0.f, s1 = 0.f, s2 = 0.f, s3 = 0.f;
#pragma unroll 1
    for (int j = 0; j < (bt < 2 ? 16 : 8); j++) {
      const int je = j < 12 ? bt * 12 + j : 20 + bt * 4 + j;
      const u32 la = __builtin_amdgcn_raw_buffer_load_b16(rw, (je >> 3) * 1024 + lane * 16 + (je & 7) * 2, sl * 5120, 16) * 8u;
      const v2u v = grow(la, scur);
      s0 += bflo(v[0]); s1 += bfhi(v[0]); s2 += bflo(v[1]); s3 += bfhi(v[1]);
    }
    float a = 0.f;
#pragma unroll
    for (int c = 0; c < 4; c++) {
      const int sl_ = (lane & 48) + 4 * c + ((lane >> 2) & 3);
      const float t0 = __shfl(s0, sl_), t1 = __shfl(s1, sl_), t2 = __shfl(s2, sl_), t3 = __shfl(s3, sl_);
      a += (lane & 2) ? ((lane & 1) ? t3 : t2) : ((lane & 1) ? t1 : t0);
    }
    if (bt == 2) a += __shfl_xor(a, 32);
    const u32 n0 = slot * 40 + bt * 16;
    for (u32 i = 0; i < novf; i++) {
      const u32 es = __builtin_amdgcn_raw_buffer_load_b32(rw, i * 4, ovb, 16);
      if ((es >> 16) - n0 == (u32)(lane >> 2) && (bt < 2 || lane < 32)) {
        const v2u v = grow((es & 0xffffu) * 8, scur);
        const u32 q = (lane & 2) ? v[1] : v[0];
        a += (lane & 1) ? bfhi(q) : bflo(q);
      }
    }
    return a;
  };
  auto tail = [&](const int bt, const v4f acc, const u32 scur, const u32 voff, const u32 soff) __attribute__((always_inline)) {
    float a = acc[0] + acc[1];
    if (__builtin_expect(__builtin_amdgcn_ballot_w64(a != a) != 0, 0)) a = fallback(bt, scur);
    const float d2 = dv2[bt];
    const float tn = __builtin_bit_cast(float, DPPU(__float_as_uint(a), 0xB1)) * d2;
    const u32 cm = min(pk2(a * d2, tn), 0xFFFFFFFEu);
    asm volatile("s_mov_b64 exec, %4\n\tbuffer_store_dword %0, %1, %2, %3 offen sc1\n\ts_mov_b64 exec, -1"
                 :: "v"(cm), "v"(voff), "s"(srd), "s"(soff), "s"(bt < 2 ? 0x5555555555555555ull : 0x55555555ull) : "memory");
  };
  v4f acc = {0, 0, 0, 0}, acc2 = {0, 0, 0, 0};
  u32 vp2x = OOB;
  auto tokdma = [&](u32 id) __attribute__((always_inline)) {
    u32 lane = t;
    asm volatile("" : "+s"(id), "+v"(lane));
    lane &= 63;
    const u32 k = ((id * 205u) >> 11) + 1, rem = id - (k - 1) * 10, u = rem >> 1, h = rem & 1;
    const u32 vo = ((lane >> 2) ^ ((k >> 2) << 1)) * GSTRIDE + ((lane ^ k) & 3) * 16;
    const u32 so = WS_G + k * GBUF + h * 16 * GSTRIDE + (u < 4 ? (u >> 1) * 32768 + 128 * bs + (u & 1) * 64 : 65536 + 64 * bs);
    DMA(vo, so, chunk(u * 8 + k) + h * 1024);
  };
  const bool rdo = bs < 250;
  auto phase = [&](auto GBc, auto XMc, const bool recv, const u32 sprev, const u32 scur, const u32 srecv_, const int sb) __attribute__((always_inline)) {
    const u32 srecv = srecv_;
    constexpr u32 GB = decltype(GBc)::value;
    constexpr bool XM = decltype(XMc)::value;
    constexpr u32 to = GB * 40960u, ro = (1 - GB) * 40960u;
    constexpr int NQ = XM ? 10 : 8, E0 = XM ? 3 : 2, E1 = XM ? 7 : 5;
    const bool pois = GB == 0 && scur < sgA + (LASTP - 2) * GBUF && w < 10;
    __builtin_amdgcn_s_setprio(3);
    u32 flg = 0;
    auto vfl = [&]() { u32 l = lane; OPQ(l); return WS_FLG + (l < 16 ? p * 16 + l : 250 + (l & 1)) * 4; };
    if (GB == 0 && scur == sgA + 5 * GBUF && w == 0) {
      u32 l = lane; OPQ(l);
      asm volatile("buffer_store_dword %0, %0, %1, %2 offen sc1" :: "v"(l == 0 ? 0u : OOB), "s"(srd), "s"(WS_FLG + bs * 4) : "memory");
    }
    if (GB == 0 && scur == sgA + 6 * GBUF) asm volatile("buffer_load_dword %0, %1, %2, 0 offen sc1" : "=v"(flg) : "v"(vfl()), "s"(srd) : "memory");
    if (GB == 1 && !recv && rdo) { PF0 }
    if (recv) { DMA(vla, srecv, lb0 + ro); DMA(vla, srecv + 16384, lb0 + 16384 + ro); }
    auto grp = [&](const int s) __attribute__((always_inline)) -> v16bf {
      if (XM && (s == E0 || s == E1)) {
        u32 q0 = ex[s == E0 ? 0 : 2], q1 = ex[s == E0 ? 1 : 3];
        asm volatile("" : "+v"(q0), "+v"(q1));
        return rd4((q0 & 0xffffu) * 8 + to, (q0 >> 16) * 8 + to, (q1 & 0xffffu) * 8 + to, (q1 >> 16) * 8 + to);
      }
      const int q = XM ? s - (s > E0) - (s > E1) : s;
      return rd4(ad[4 * q] + to, ad[4 * q + 1] + to, ad[4 * q + 2] + to, ad[4 * q + 3] + to);
    };
    v16bf g[3];
#pragma unroll
    for (int q = 0; q < 3; q++) g[q] = grp(q);
    __builtin_amdgcn_sched_barrier(0);
    if (D23POS < 0 && recv) { DMA(vla, srecv + 32768, lb2 + ro); DMA(vla, srecv + 49152, lb0 + 90112 + ro); }
    float e0 = 0.f, e1 = 0.f, e2 = 0.f;
    if (__builtin_expect(novf != 0, 0)) { e0 = slow(to, slot * 40, 16); e1 = slow(to, slot * 40 + 16, 16); e2 = slow(to, slot * 40 + 32, 8); }
    __builtin_amdgcn_sched_barrier(0);
    u32 chk = 0;
#pragma unroll
    for (int q = 0; q < NQ; q++) {
      if (q == 0) { acc[0] = e0; acc[1] = 0.f; }
      if (q == E0 + 1) { acc[0] = e1; acc[1] = 0.f; }
      if (q == E1 + 1) { acc2[0] = e2; acc2[1] = 0.f; }
      if (q == NQ - 1 && recv) {
        asm volatile("s_waitcnt vmcnt(%0)" :: "n"(D4POS <= E0 ? 2 : 1) : "memory");
        chk = *(volatile LAS u32*)(size_t)(vchk + ro);
      }
      if (q <= E1) acc = __builtin_amdgcn_smfmac_f32_16x16x64_bf16(sel1, g[q % 3], acc, seli, 0, 0);
      else acc2 = __builtin_amdgcn_smfmac_f32_16x16x64_bf16(sel2, g[q % 3], acc2, seli, 0, 0);
      if (q + 3 < NQ) g[q % 3] = grp(q + 3);
      if (q == D23POS && recv) { DMA(vla, srecv + 32768, lb2 + ro); DMA(vla, srecv + 49152, lb0 + 90112 + ro); }
      if (q == D4POS && recv) DMA(vlc, srecv + 65536, lb0 + 106496 + ro);
      if (GB == 1 && !recv && rdo) { if (q == 0) { PF1 } }
      if (q == PR1) __builtin_amdgcn_s_setprio(2);
      if (q == PR2) __builtin_amdgcn_s_setprio(1);
      if (q == PR3) __builtin_amdgcn_s_setprio(0);
      __builtin_amdgcn_sched_barrier(0);
      if (q == E0 || q == E1) {
        tail(q == E1, acc, scur, vp01, scur + GBUF + (q == E1 ? 32768 : 0));
        if (GB == 1 && !recv && rdo) { if (q == E0) { PF2 } else { PF3 } }
        __builtin_amdgcn_sched_barrier(0);
      }
    }
    auto again = [&](const u64 bad) __attribute__((always_inline)) {
      if (bad & 0xffull) DMA(vla, srecv, lb0 + ro);
      if (bad & 0xff00ull) DMA(vla, srecv + 16384, lb0 + 16384 + ro);
      if (bad & 0xff0000ull) DMA(vla, srecv + 32768, lb2 + ro);
      if (bad & 0xff000000ull) DMA(vla, srecv + 49152, lb0 + 90112 + ro);
      if (bad >> 32) DMA(vlc, srecv + 65536, lb0 + 106496 + ro);
    };
    u64 bad = __builtin_amdgcn_ballot_w64(chk == POISON);
    if (__builtin_expect(bad != 0, 0)) again(bad);
    tail(2, acc2, scur, vp2, scur + GBUF);
    if (pois) {
      const v4u pz = {POISON, POISON, POISON, POISON};
      asm volatile("buffer_store_dwordx4 %0, %1, %2, %3 offen sc1" :: "v"(pz), "v"(vpz), "s"(srd), "s"(scur + 3 * GBUF) : "memory");
    }
    PSTAMP(sb);
    if (recv) {
      u32 spins = 0;
      if (GB == 0) {
        asm volatile("" : "+v"(flg));
        for (u32 sp = 0; __builtin_amdgcn_ballot_w64(flg == POISON) != 0 && sp < MAXSPIN; sp++)
          asm volatile("buffer_load_dword %0, %1, %2, 0 offen sc1\n\ts_waitcnt vmcnt(0)" : "=v"(flg) : "v"(vfl()), "s"(srd) : "memory");
      }
      if (__builtin_expect(bad != 0, 0)) {
        if (pois) asm volatile("s_waitcnt vmcnt(2)" ::: "memory"); else asm volatile("s_waitcnt vmcnt(1)" ::: "memory");
        bad = __builtin_amdgcn_ballot_w64(*(volatile LAS u32*)(size_t)(vchk + ro) == POISON);
        spins++;
        while (bad != 0 && spins < MAXSPIN) {
          spins++;
          again(bad);
          asm volatile("s_waitcnt vmcnt(0)" ::: "memory");
          bad = __builtin_amdgcn_ballot_w64(*(volatile LAS u32*)(size_t)(vchk + ro) == POISON);
        }
      }
      PSTAMP(sb + 1);
      asm volatile("" ::: "memory");
      __builtin_amdgcn_s_barrier();
      asm volatile("" ::: "memory");
      PSTAMP(sb + 2);
    }
  };
  using F0 = std::integral_constant<u32, 0>; using F1 = std::integral_constant<u32, 1>;
  auto hops = [&](auto XMc) __attribute__((always_inline)) {
    u32 sk = sgA;
#pragma unroll 1
    for (int k = 1; k <= KMAX; k++) {
      phase(F0{}, XMc, true, sk - GBUF + GSTRIDE, sk, sk + GSTRIDE, 6 * k - 4);
      phase(F1{}, XMc, k < KMAX, sk, sk + GSTRIDE, sk + GBUF, 6 * k - 1);
      sk += GBUF;
    }
  };
  if (xm) hops(std::true_type{}); else hops(std::false_type{});
  STAMP(47);
  if (rdo) {
    PF4
    v4u xv = {0, 0, 0, 0};
    if (t < 640) xv = *(const v4u*)(x + (size_t)bs * 5120 + t * 8);
    const u32 j0 = w < 8 ? 2 * w : min(w + 8, 19), two = w < 8 ? 2 : 0;
    const u16 dq0 = dis[bs * 40 + 2 * j0 + (lane & 1)], dq1 = dis[bs * 40 + 2 * j0 + two + (lane & 1)];
    LBAR();
    STAMP(58);
    DMA(lane * 16, WS_W + w * 1024, L_WM + w * 1024); DMA(lane * 16, WS_W + 16384 + w * 1024, L_WM + 16384 + w * 1024);
    if (w < 4) DMA(lane * 16, WS_W + 65536 + w * 1024, L_WM + L_WR + w * 1024);
    if (t == 0) *(volatile LAS u32*)(size_t)L_FLAG = 0;
    if (w >= 6) tokdma(w + 54);
    if (t < 640) {
      const u32 node = t >> 4, a = chunk((node >> 3) * 8) + (t & 15) * 128 + ((node >> 1) & 3) * 16 + (node & 1) * 8;
      *(LAS v2u*)(size_t)a = v2u{xv[0], xv[1]}; *(LAS v2u*)(size_t)(a + 64) = v2u{xv[2], xv[3]};
    }
    const TfPre P = tfpre(j0, bs, lane);
    float dv0 = bf2f(dq0), dv1 = bf2f(dq1);
    dv0 = dv0 == 0.f ? 1.f : dv0; dv1 = dv1 == 0.f ? 1.f : dv1; OPQ2(dv0, dv1);
    for (u32 spins = 0;; spins++) {
      if (spins == 0) STAMP(56);
      asm volatile("s_waitcnt vmcnt(0)" ::: "memory");
      if (spins == 0) STAMP(59);
      u32 bad = 0; v4u cv[5];
#pragma unroll
      for (int i = 0; i < 5; i++) {
        const u32 id = i < 3 ? w + 16 * i : (i == 3 ? (w < 12 ? w + 48 : w) : (w >= 6 ? w + 54 : w));
        const u32 k = ((id * 205u) >> 11) + 1, rem = id - (k - 1) * 10;
        cv[i] = ld128(chunk((rem >> 1) * 8 + k) + (rem & 1) * 1024 + lane * 16);
      }
      SB();
#pragma unroll
      for (int i = 0; i < 5; i++) bad |= __builtin_amdgcn_ballot_w64(max(max(cv[i][0], cv[i][1]), max(cv[i][2], cv[i][3])) == POISON) != 0 ? 1u << i : 0u;
      if (bad == 0 || spins >= MAXSPIN) break;
#pragma unroll
      for (int i = 0; i < 5; i++)
        if ((bad >> i) & 1) tokdma(i < 3 ? w + 16 * i : (i == 3 ? (w < 12 ? w + 48 : w) : (w >= 6 ? w + 54 : w)));
    }
    STAMP(46);
    LBAR();
    STAMP(60);
    if (w < 8) tf<2, P3A>(P, lane, dv0, dv1, out);
    else if (w < 12) tf<1, P3C>(P, lane, dv0, dv1, out);
    else {
#pragma unroll
      for (int i = 0; i < 8; i++) DMA(lane * 16, WS_W + 32768 + (i * 4 + w - 12) * 1024, L_WM + L_WW + (i * 4 + w - 12) * 1024);
      asm volatile("s_waitcnt vmcnt(0)" ::: "memory");
      if (lane == 0) __hip_atomic_fetch_add((LAS u32*)(size_t)L_FLAG, 1u, __ATOMIC_RELAXED, __HIP_MEMORY_SCOPE_WORKGROUP);
    }
    STAMP(61);
  }
  STAMP_END;
  asm volatile("L_k2_last:");
#undef KID
}
__device__ __host__ __forceinline__ u32 rowof(u32 n) { u32 s = n / 40, l = n - s * 40; return l < 32 ? (l >> 4) * 4096 + 16 * s + (l & 15) : 8192 - 32 + 8 * s + l; }

extern "C" void kernel_launch(void* const* d_in, const int* in_sizes, int n_in, void* d_out, int out_size, void* d_ws, size_t ws_size, hipStream_t stream) {
  const u16* x = (const u16*)d_in[0]; const int* src = (const int*)d_in[1]; const int* dst = (const int*)d_in[2]; const u16* dis = (const u16*)d_in[3];
  const u16* wq = (const u16*)d_in[4]; const u16* wk = (const u16*)d_in[5]; const u16* wv = (const u16*)d_in[6]; const u16* wo = (const u16*)d_in[7]; const u16* wr = (const u16*)d_in[8];
  char* ws = (char*)d_ws;
  k1<<<253, 640, 0, stream>>>(src, dst, x, dis, wq, wk, wv, wo, ws, (const u16*)d_out);
  k2<<<256, 1024, 0, stream>>>(dis, wq, wk, wv, wo, wr, x, ws, (u16*)d_out);
}
#define WS_TOK WS_G
#define TOKBYTES (8u * GBUF)
#define TOKSC(n) hbf2f(dis[n])
#define TOKADDR(n,k,d) ((size_t)(k) * GBUF + ((d) >> 2) * GSTRIDE + rowof(n) * 8 + ((d) & 3) * 2)
```

```cpp
#include <hip/hip_runtime.h>
#include <hip/hip_bf16.h>
#include <stdint.h>
#include <type_traits>

#define NN 10000
#define EE 320000
typedef unsigned short u16;
typedef unsigned u32;
typedef unsigned long long u64;
typedef u32 v4u __attribute__((ext_vector_type(4)));
typedef u32 v2u __attribute__((ext_vector_type(2)));
typedef short v4s __attribute__((ext_vector_type(4)));
typedef float v4f __attribute__((ext_vector_type(4)));
typedef __bf16 v4bf __attribute__((ext_vector_type(4)));
typedef __bf16 v8bf __attribute__((ext_vector_type(8)));
typedef __bf16 v16bf __attribute__((ext_vector_type(16)));
typedef __bf16 v2bf __attribute__((ext_vector_type(2)));
typedef float v2f __attribute__((ext_vector_type(2)));
typedef u16 v2h __attribute__((ext_vector_type(2)));
#define LAS __attribute__((address_space(3)))

#define GSTRIDE 81920u
#define GBUF    (32u * GSTRIDE)
#define WS_G    0u
#define WS_SUB  (8u * GBUF)
#define WS_ELL  (WS_SUB + 4096000u)
#define WS_HDR  (WS_ELL + 1280000u)
#define HS 128u
#define WS_FLG  (WS_HDR + 256u * HS)
#define WS_CNT  (WS_FLG + 1024u)
#define WS_OVF1 (WS_CNT + 256u)
#define WS_OVF2 (WS_OVF1 + 128u * 2504u * 4u)
#define WS_W    (WS_OVF2 + 1280000u)
#define WS_DBG  (WS_W + 69632u)
#define POISON 0xFFFFFFFFu
#define OOB 0x40000000u
#define ZROW 15312u
#define MAXSPIN 100000u
#define AUX1 16

#define WST
#define WSA
__device__ __forceinline__ float bf2f(u16 v) { return __uint_as_float(((u32)v) << 16); }
__device__ __forceinline__ float bflo(u32 v) { return __uint_as_float(v << 16); }
__device__ __forceinline__ float bfhi(u32 v) { return __uint_as_float(v & 0xffff0000u); }
__device__ __forceinline__ u32 pk2(float a, float b) { v2f f = {a, b}; v2bf r = __builtin_convertvector(f, v2bf); return __builtin_bit_cast(u32, r); }
__device__ __forceinline__ u32 pk2s(float a, float b) { return min(pk2(a, b), 0xFFFFFFFEu); }
#define DPPU(v, ctrl) ((u32)__builtin_amdgcn_mov_dpp((int)(v), (ctrl), 0xF, 0xF, true))
#define LBAR() do { asm volatile("" ::: "memory"); __builtin_amdgcn_s_waitcnt(0xC07F); __builtin_amdgcn_s_barrier(); asm volatile("" ::: "memory"); } while (0)
__device__ __forceinline__ u32 tabidx(u32 n) {
  u32 s = (n * 52429u) >> 21, l = n - s * 40u;
  u32 row = l < 32u ? ((l >> 4) << 12) + 16u * s + (l & 15u) : 8192u - 32u + 8u * s + l;
  return row + (row >= 5120u ? 5120u : 0u);
}

#define DMA0(voff, soff, lbase) asm volatile("s_mov_b32 m0, %2\n\ts_nop 0\n\tbuffer_load_dwordx4 %0, %3, %1 offen lds" :: "v"(voff), "s"(soff), "s"(lbase), "s"(srd) : "memory")
__device__ __forceinline__ v4bf rd(u32 a) { return __builtin_amdgcn_ds_read_tr16_b64_v4bf16((LAS v4bf*)(size_t)a); }
#define MFMA(a, b, c) __builtin_amdgcn_mfma_f32_16x16x32_bf16(__builtin_bit_cast(v8bf, (v4u)(a)), __builtin_bit_cast(v8bf, (v4u)(b)), (c), 0, 0, 0)
__device__ __forceinline__ void sw16(float& a, float& b) { const v2u r = __builtin_amdgcn_permlane16_swap(__float_as_uint(a), __float_as_uint(b), false, false); a = __uint_as_float(r[0]); b = __uint_as_float(r[1]); }
__device__ __forceinline__ void sw32(float& a, float& b) { const v2u r = __builtin_amdgcn_permlane32_swap(__float_as_uint(a), __float_as_uint(b), false, false); a = __uint_as_float(r[0]); b = __uint_as_float(r[1]); }
#define DPPF(v, ctrl) __uint_as_float(DPPU(__float_as_uint(v), ctrl))
#define LOG2E 1.4426950408889634f
template <class F>
__device__ __forceinline__ void wprep(const int which, const u16* wq, const u16* wk, const u16* wv, const u16* wo, const u16* wr, char* ws, float* L, F&& early WST) {
  const int t = threadIdx.x, lane = t & 63, m = __builtin_amdgcn_readfirstlane(t >> 6), g = lane >> 4, i = lane & 15;
  v4f D[8];
  v4u B[4];
  const __amdgpu_buffer_rsrc_t rsw = __builtin_amdgcn_make_buffer_rsrc(ws + WS_W, 0, 69632, 0x00020000);
  const float wra = bf2f(wr[lane]), wrb = bf2f(wr[64 + lane]), wrm = bf2f(wr[16 * (m & 7) + i]);
  const u32 lb = (u32)(size_t)(LAS float*)L;
  if (m < 8) {
    const u16* wa = which == 0 ? wq : wv;
    {
      const v4u srd = {(u32)(size_t)wa, (u32)((size_t)wa >> 32) & 0xffffu, 32768u, 0x00020000u};
#pragma unroll
      for (int ks = 0; ks < 4; ks++) DMA0(i * 256 + g * 16, m * 4096 + ks * 64, lb + (m * 4 + ks) * 1024);
    }
    if (which == 0) {
#pragma unroll
      for (int ks = 0; ks < 4; ks++) B[ks] = *(const v4u*)(wk + (16 * m + i) * 128 + 32 * ks + 8 * g);
    } else {
      const v4u srd = {(u32)(size_t)wo, (u32)((size_t)wo >> 32) & 0xffffu, 32768u, 0x00020000u};
#pragma unroll
      for (int ks = 0; ks < 4; ks++) DMA0(lane * 16, (m * 4 + ks) * 1024, lb + 81920 + (m * 4 + ks) * 1024);
    }
  }
  asm volatile("" ::: "memory");
  __builtin_amdgcn_s_waitcnt(0x0070);
  __builtin_amdgcn_s_barrier();
  asm volatile("" ::: "memory");
  early();
  if (m < 8) {
    if (which == 1) {
#pragma unroll
      for (int ks = 0; ks < 4; ks++) {
        const u32 a = lb + 81920 + (32 * ks + 8 * g + (i >> 2)) * 256 + m * 32 + (i & 3) * 8;
        const v2u lo = __builtin_bit_cast(v2u, rd(a)), hi = __builtin_bit_cast(v2u, rd(a + 1024));
        B[ks] = v4u{lo[0], lo[1], hi[0], hi[1]};
      }
    }
    float cs = 0.f;
#pragma unroll
    for (int dt = 0; dt < 8; dt++) {
      v4f a = {0, 0, 0, 0};
#pragma unroll
      for (int ks = 0; ks < 4; ks++) a = MFMA(*(const LAS v4u*)(size_t)(lb + (dt * 4 + ks) * 1024 + lane * 16), B[ks], a);
      D[dt] = a; cs += (a[0] + a[1]) + (a[2] + a[3]);
    }
    float c2 = cs; sw16(cs, c2); cs += c2; c2 = cs; sw32(cs, c2); cs = (cs + c2) * (1.f / 128.f);
#pragma unroll
    for (int dt = 0; dt < 8; dt++) D[dt] -= cs;
    float wc = 0.f;
    if (which == 1) {
      float z = wra + wrb; z += DPPF(z, 0x121); z += DPPF(z, 0x122); z += DPPF(z, 0x124); z += DPPF(z, 0x128);
      c2 = z; sw16(z, c2); z += c2; c2 = z; sw32(z, c2); z = (z + c2) * (1.f / 128.f);
      wc = (wrm - z) * LOG2E;
      if (g == 0) L[28672 + 16 * m + i] = wc;
    }
#pragma unroll
    for (int pass = 0; pass < 2; pass++) {
      if (pass == 0 || which == 1) {
        v4f r[8];
#pragma unroll
        for (int dt = 0; dt < 8; dt++) r[dt] = pass ? D[dt] * wc : D[dt];
        __builtin_amdgcn_sched_barrier(0);
#pragma unroll
        for (int st = 0; st < 4; st++) {
#pragma unroll
          for (int dt = 0; dt < 8; dt++)
#pragma unroll
            for (int q = 0; q < 4; q++) { const float z = r[dt][q]; r[dt][q] = z + (st == 0 ? DPPF(z, 0x121) : st == 1 ? DPPF(z, 0x122) : st == 2 ? DPPF(z, 0x124) : DPPF(z, 0x128)); }
          __builtin_amdgcn_sched_barrier(0);
        }
        if (i == 0) {
#pragma unroll
          for (int dt = 0; dt < 8; dt++) *(v4f*)(L + 8192 + pass * 1024 + m * 128 + dt * 16 + 4 * g) = r[dt];
        }
      }
    }
  }
  LBAR();
  if (t < 256) {
    float z = 0.f;
#pragma unroll
    for (int q = 0; q < 8; q++) z += L[8192 + (t >> 7) * 1024 + q * 128 + (t & 127)];
    L[t] = z;
  }
  asm volatile("s_waitcnt vmcnt(0)" ::: "memory");
  LBAR();
  if (which == 0) {
    if (m < 8) {
#pragma unroll
      for (int dt = 0; dt < 8; dt++) D[dt] = (D[dt] - *(const v4f*)(L + dt * 16 + 4 * g) * (1.f / 128.f)) * (LOG2E * 0.08838834764831845f);
    }
  } else if (m < 4) {
    const float* p = L + ((i & 3) == 1 ? 28672 : ((i & 1) ? 128 : 0)) + 32 * m + 4 * g;
    v4f a = *(const v4f*)p, c = *(const v4f*)(p + 16);
    v4u o = {pk2(a[0], a[1]), pk2(a[2], a[3]), pk2(c[0], c[1]), pk2(c[2], c[3])};
    if ((i & 7) == 5) o = v4u{pk2(a[0] - bflo(o[0]), a[1] - bfhi(o[0])), pk2(a[2] - bflo(o[1]), a[3] - bfhi(o[1])), pk2(c[0] - bflo(o[2]), c[1] - bfhi(o[2])), pk2(c[2] - bflo(o[3]), c[3] - bfhi(o[3]))};
    if ((i & 3) == 0) o = v4u{0x3f803f80u, 0x3f803f80u, 0x3f803f80u, 0x3f803f80u};
    __builtin_amdgcn_raw_buffer_store_b128(o, rsw, 65536 + m * 1024 + lane * 16, 0, 16);
  }
  if (m < 8) {
#pragma unroll
    for (int s = 0; s < 4; s++) {
      const v4u o = {pk2(D[2 * s][0], D[2 * s][1]), pk2(D[2 * s][2], D[2 * s][3]), pk2(D[2 * s + 1][0], D[2 * s + 1][1]), pk2(D[2 * s + 1][2], D[2 * s + 1][3])};
      __builtin_amdgcn_raw_buffer_store_b128(o, rsw, which * 32768 + (m * 4 + s) * 1024 + lane * 16, 0, 16);
    }
  }
}

#define TLBS 524288
#define K1P 2
#define K1AUX 16
#define K2SYM "_Z2k2PKtS0_S0_S0_S0_S0_S0_PcPt"
__global__ void __launch_bounds__(640) k1(const int* __restrict__ src, const int* __restrict__ dst, const u16* __restrict__ x,
                                          const u16* __restrict__ dis, const u16* __restrict__ wq, const u16* __restrict__ wk,
                                          const u16* __restrict__ wv, const u16* __restrict__ wo, char* __restrict__ ws, const u16* __restrict__ out) {
#define KID 0
  __shared__ __attribute__((aligned(16))) u32 L[8704];
  const int t = threadIdx.x, b = blockIdx.x;
  const __amdgpu_buffer_rsrc_t rs = __builtin_amdgcn_make_buffer_rsrc(ws, 0, WS_DBG, 0x00020000);
  if (b < 128) {
    u32* stage = L; u32* cnt = L + 8000; u32* novf = L + 8256;
    v4u s4 = {0, 0, 0, 0}, d4 = {0, 0, 0, 0};
    if (t < 625) { s4 = *(const v4u*)(src + b * 2500 + t * 4); d4 = *(const v4u*)(dst + b * 2500 + t * 4); }
    u32 pf = 0;
    if (t >= 632) pf = *(const u32*)(((b & 64) ? ((b & 32) ? wo : wv) : ((b & 32) ? wk : wq)) + (b & 31) * 512 + (t - 632) * 64);
    if (t < 257) cnt[t] = 0;
    __syncthreads();
    u32* ovf = (u32*)(ws + WS_OVF1) + b * 2504;
    if (t < 625) {
      u32 r[4], bin[4], en[4];
#pragma unroll
      for (int i = 0; i < 4; i++) {
        u32 d = d4[i];
        bin[i] = (d * 52429u) >> 21;
        r[i] = atomicAdd(&cnt[bin[i]], 1u);
        en[i] = tabidx(s4[i]) | ((d - bin[i] * 40u) << 16);
      }
#pragma unroll
      for (int i = 0; i < 4; i++) {
        if (r[i] < 31) stage[bin[i] * 32 + 1 + r[i]] = en[i];
        else { u32 o = atomicAdd(novf, 1u); ovf[4 + o] = (en[i] & 0xffffu) | (d4[i] << 16); }
      }
    }
    __syncthreads();
    if (t == 0) ovf[0] = *novf;
#pragma unroll
    for (int it = 0; it < 4; it++) {
      int i = t + it * 640;
      if (i < 2000) {
        int bin = i >> 3, q = i & 7;
        u32 c = cnt[bin];
        if ((u32)((q & ~7) * 4) <= c) {
          v4u v = *(const v4u*)(stage + bin * 32 + q * 4);
          if (q == 0) v[0] = c;
          __builtin_amdgcn_raw_buffer_store_b128(v, rs, WS_SUB + b * 128 + bin * 16384 + q * 16, 0, AUX1);
        }
      }
    }
    if (b == 0 && t == 0) *(u32*)(ws + WS_CNT) = 0;
    asm volatile("" :: "v"(pf));
  } else {
    const int wb = b - 128;
    char* S = (char*)L;
    v4u v[2]; float dv[2]; u32 go[2]; u16 dh[2];
#pragma unroll
    for (int it = 0; it < 2; it++) {
      int i = t + it * 640; int node = i >> 4, cq = i & 15;
      v[it] = *(const v4u*)(x + (size_t)(wb * 80 + node) * 128 + cq * 8);
      dh[it] = dis[wb * 80 + node];
    }
#pragma unroll
    for (int it = 0; it < 2; it++) {
      int i = t + it * 640; int g = i / 40, pc = i - g * 40;
      go[it] = g * GSTRIDE + (pc < 32 ? (pc >> 4) * 32768 + wb * 256 + (pc & 15) * 16 : 65536 + wb * 128 + (pc - 32) * 16);
    }
#pragma unroll
    for (int it = 0; it < 2; it++) {
      int i = t + it * 640; int node = i >> 4, cq = i & 15;
      int s = node >= 40, l = node - s * 40;
      dv[it] = bf2f(dh[it]);
      int pos = l < 32 ? (l >> 4) * 32 + s * 16 + (l & 15) : 32 + s * 8 + l;
      v2u a, c;
      a[0] = pk2s(bflo(v[it][0]) * dv[it], bfhi(v[it][0]) * dv[it]);
      a[1] = pk2s(bflo(v[it][1]) * dv[it], bfhi(v[it][1]) * dv[it]);
      c[0] = pk2s(bflo(v[it][2]) * dv[it], bfhi(v[it][2]) * dv[it]);
      c[1] = pk2s(bflo(v[it][3]) * dv[it], bfhi(v[it][3]) * dv[it]);
      *(v2u*)(S + (2 * cq) * 656 + pos * 8) = a;
      *(v2u*)(S + (2 * cq + 1) * 656 + pos * 8) = c;
    }
    __syncthreads();
#pragma unroll
    for (int it = 0; it < 2; it++) {
      int i = t + it * 640; int g = i / 40, pc = i - g * 40;
      v4u o = *(const v4u*)(S + g * 656 + pc * 16);
      __builtin_amdgcn_raw_buffer_store_b128(o, rs, WS_G + go[it], 0, AUX1);
    }
    const v4u pz = {POISON, POISON, POISON, POISON};
#pragma unroll
    for (int it = 0; it < K1P; it++) __builtin_amdgcn_raw_buffer_store_b128(pz, rs, WS_G + go[it & 1], (1 + (it >> 1)) * GBUF, K1AUX);
    __builtin_amdgcn_raw_buffer_store_b128(pz, rs, WS_ELL + wb * 10240 + t * 16, 0, 16);
    if (t < 17) __builtin_amdgcn_raw_buffer_store_b128(pz, rs, t < 16 ? WS_HDR + wb * 256 + t * 16 : (wb < 64 ? WS_FLG + wb * 16 : OOB), 0, 16);
  }
#undef KID
}

#define STRICTR 0
#define B_ELL 0
#define B_CB 1280
#define B_CN 1472
#define B_BUSY 2240
#define B_FULL 2432
#define B_ROWM 2480
#define B_NT 2576
#define B_NF 2577
#define B_Z0 1280
#define B_ZN 1304
#define B_Q 2592
#define B_F 20480
#define FID t
#define B_N1 2578
#define B_NM 2579
#define B_NO 2580
#define B_M2 2581
#define B_BASE 2582
template <class F, class F2>
__device__ __forceinline__ void build(const int slot, char* __restrict__ ws, u32* L, F&& early, F2&& early2 WST) {
  const int t = threadIdx.x, lane = t & 63, w = __builtin_amdgcn_readfirstlane(t >> 6);
  const __amdgpu_buffer_rsrc_t rs = __builtin_amdgcn_make_buffer_rsrc(ws, 0, WS_DBG, 0x00020000);
  v4u v; u32 n1;
  {
    const v4u srd = {(u32)(size_t)ws, (u32)((size_t)ws >> 32) & 0xffffu, WS_DBG, 0x00020000u};
    asm volatile("buffer_load_dwordx4 %0, %2, %4, %5 offen\n\tbuffer_load_dword %1, %3, %4, 0 offen"
                 : "=&v"(v), "=&v"(n1) : "v"(t * 16), "v"(t < 128 ? WS_OVF1 + t * 10016u : OOB), "s"(srd), "s"(WS_SUB + slot * 16384) : "memory");
  }
  {
    u32 lo, hi, sz;
    asm volatile("s_getpc_b64 vcc\nL_pc%=:\n\ts_sub_u32 %0, vcc_lo, L_pc%=-" K2SYM "\n\ts_subb_u32 %1, vcc_hi, 0\n\ts_mov_b32 %2, L_k2_last-" K2SYM
                 : "=&s"(lo), "=&s"(hi), "=s"(sz) :: "vcc", "scc");
    const v4u sc = {lo, hi & 0xffffu, sz, 0x00020000u};
    asm volatile("s_mov_b32 m0, 0x26400\n\ts_nop 0\n\tbuffer_load_dword %0, %1, 0 offen lds" :: "v"(t < 14 ? ((blockIdx.x >> 3) * 14 + t) * 128 : OOB), "s"(sc) : "memory");
  }
#define SUBW "6"
  if (t < 320) { const v4u z = {POISON, POISON, POISON, POISON}; *(v4u*)(L + B_ELL + t * 4) = z; }
  else if (t < 320 + B_ZN / 4) { const v4u z = {0, 0, 0, 0}; *(v4u*)(L + B_Z0 + (t - 320) * 4) = z; }
  LBAR();
  early();
  asm volatile("s_waitcnt vmcnt(" SUBW ")" : "+v"(v), "+v"(n1) :: "memory");
  {
    const u32 q = t & 7;
    u32 c = DPPU(v[0], 0x00);
    c = (u32)__builtin_amdgcn_update_dpp((int)c, (int)c, 0x114, 0xF, 0xA, false);
    c = min(c, 31u);
    u32 inc = c + (u32)__builtin_amdgcn_update_dpp(0, (int)c, 0x118, 0xF, 0xC, false);
    inc += (u32)__builtin_amdgcn_update_dpp(0, (int)inc, 0x142, 0xA, 0xF, false);
    inc += (u32)__builtin_amdgcn_update_dpp(0, (int)inc, 0x143, 0xC, 0xF, false);
    u32 base = 0;
    if (lane == 63) base = atomicAdd(&L[B_NT], inc);
    base = __builtin_amdgcn_readlane(base, 63);
    const u32 pos = B_Q + base + inc - c + 4 * q - 1;
#pragma unroll
    for (int i = 0; i < 4; i++) {
      const u32 id = 4 * q + i;
      L[(id - 1u < c) ? pos + i : B_Q + 4096 + lane] = v[i];
    }
  }
  if (n1) atomicAdd(&L[B_N1], n1);
  early2();
  LBAR();
  auto place = [&](u32 e, u32 r, u32 c) {
    const u32 loc = e >> 16, hp = loc >> 3;
    const u32 j = hp == 4 ? 24 + (r >> 1) : (r < 12 ? (hp >> 1) * 12 + r : 20 + (hp >> 1) * 4 + r);
    const u32 half = (hp == 4 ? r : hp) & 1;
    const u32 ln = half * 32 + (loc & 4) * 4 + c * 4 + (loc & 3);
    ((u16*)(L + B_ELL))[(j >> 3) * 512 + ln * 8 + (j & 7)] = (u16)e;
    atomicOr(&L[B_ROWM + half * 40 + j], 1u << ((e + 16) & 31));
  };
  const u32 tot = L[B_NT];
  for (u32 b0 = w * 128; b0 < tot; b0 += 2048) {
    u32 e[2], hb[2], rk[2], r[2], c[2];
#pragma unroll
    for (int i = 0; i < 2; i++) { const u32 id = b0 + i * 64 + lane; e[i] = L[B_Q + id]; if (id >= tot) e[i] = ((40 + (lane & 7)) << 16) | (lane & 31); }
#pragma unroll
    for (int i = 0; i < 2; i++) { hb[i] = (e[i] >> 19) * 32 + (e[i] & 31); rk[i] = atomicAdd(&L[B_CB + hb[i]], 1u); }
#pragma unroll
    for (int i = 0; i < 2; i++) {
      r[i] = e[i] >= (32u << 16) ? (rk[i] + (e[i] >> 1)) & 15 : (rk[i] < 12 ? rk[i] : rk[i] - 12);
      c[i] = atomicAdd(&L[rk[i] < 24 ? B_CN + (e[i] >> 16) * 16 + r[i] : B_CN + 640 + lane], 1u);
    }
#pragma unroll
    for (int i = 0; i < 2; i++) {
      if (e[i] < (40u << 16)) {
        if (rk[i] < 24 && c[i] < 4) {
          place(e[i], r[i], c[i]);
          atomicOr(&L[B_BUSY + hb[i]], 1u << r[i]);
          if (c[i] == 3) atomicOr(&L[B_FULL + (e[i] >> 16)], 1u << r[i]);
        } else L[B_F + atomicAdd(&L[B_NF], 1u)] = e[i];
      }
    }
  }
  LBAR();
  const u32 nf = L[B_NF];
  for (u32 id = FID; id < nf; id += 1024) {
    const u32 e = L[B_F + id], loc = e >> 16, hb = (e >> 19) * 32 + (e & 31);
    u32 rho = (id * 5 + (id >> 2)) & 15, tried = 0;
    const u32 pm = loc >= 32 ? 0xFFFFu : 0xFFFu;
    for (int round = 0;; round++) {
      const u32 bs = __hip_atomic_load(&L[B_BUSY + hb], __ATOMIC_RELAXED, __HIP_MEMORY_SCOPE_WORKGROUP);
      const u32 nfl = ~(__hip_atomic_load(&L[B_FULL + loc], __ATOMIC_RELAXED, __HIP_MEMORY_SCOPE_WORKGROUP) | tried);
      u32 cand = nfl & ~bs & pm;
      const bool strict = cand != 0 && round < STRICTR;
      if (!cand) cand = nfl & pm;
      if (!cand) cand = nfl & ~bs & 0xF000;
      if (!cand) cand = nfl & 0xF000;
      if (!cand) { L[B_Q + atomicAdd(&L[B_NO], 1u)] = (e & 0xffffu) | ((slot * 40 + loc) << 16); break; }
      const u32 r = (rho + __builtin_ctz((cand | (cand << 16)) >> rho)) & 15;
      const u32 ob = atomicOr(&L[B_BUSY + hb], 1u << r);
      const u32 c = atomicAdd(&L[B_CN + loc * 16 + r], 1u);
      if (c == 3) atomicOr(&L[B_FULL + loc], 1u << r);
      if (c < 4 && !(strict && ((ob >> r) & 1))) { place(e, r, c); break; }
      if (c >= 3) tried |= 1u << r;
      rho = (rho + 7) & 15;
    }
  }
  auto scan = [&](auto&& fn) {
    for (int j = 0; j < 128; j++) {
      const u32* o1 = (const u32*)(ws + WS_OVF1) + j * 2504;
      const u32 n = o1[0];
      for (u32 i = t; i < n; i += 1024) { const u32 en = o1[4 + i]; if ((en >> 16) - slot * 40u < 40u) fn(en); }
    }
  };
  const bool sp = L[B_N1] != 0;
  if (sp) scan([&](u32) { atomicAdd(&L[B_NM], 1u); });
  asm volatile("s_waitcnt vmcnt(0)" ::: "memory");
  LBAR();
  const u32 no = L[B_NO], nt = no + L[B_NM];
  u32 base = 0;
  if (nt) {
    if (t == 0) L[B_BASE] = atomicAdd((u32*)(ws + WS_CNT), nt);
    LBAR();
    base = L[B_BASE];
    for (u32 i = t; i < no; i += 1024) __builtin_amdgcn_raw_buffer_store_b32(L[B_Q + i], rs, WS_OVF2 + (base + i) * 4, 0, 16);
    if (sp) scan([&](u32 en) { __builtin_amdgcn_raw_buffer_store_b32(en, rs, WS_OVF2 + (base + no + atomicAdd(&L[B_M2], 1u)) * 4, 0, 16); });
    asm volatile("s_waitcnt vmcnt(0)" ::: "memory");
    LBAR();
  }
  if (t < 320) {
    v4u o = *(const v4u*)(L + B_ELL + t * 4);
    const u32* rm = L + B_ROWM + (lane >> 5) * 40 + w * 8;
    const v4u m0 = *(const v4u*)rm, m1 = *(const v4u*)(rm + 4);
#pragma unroll
    for (int i = 0; i < 4; i++) {
      const u32 a = i < 2 ? m0[2 * i] : m1[2 * i - 4], bb = i < 2 ? m0[2 * i + 1] : m1[2 * i - 3];
      u32 fa, fb;
      asm("v_ffbl_b32 %0, %1" : "=v"(fa) : "v"(~a)); asm("v_ffbl_b32 %0, %1" : "=v"(fb) : "v"(~bb));
      v2h d = {(u16)(fa + ZROW), (u16)(fb + ZROW)};
      const u32 oi = o[i];
      o[i] = __builtin_bit_cast(u32, __builtin_elementwise_min(__builtin_bit_cast(v2h, oi), d));
    }
    __builtin_amdgcn_raw_buffer_store_b128(o, rs, WS_ELL + slot * 5120 + t * 16, 0, 16);
  }
  if ((t >> 3) == 40) __builtin_amdgcn_raw_buffer_store_b128(v4u{base, nt, base, nt}, rs, WS_HDR + slot * 128 + (t & 7) * 16, 0, 16);
}

#define L_WM 40960u
#define L_FLAG 160000u
#define L_WR 32768u
#define L_WW 81920u
__device__ __forceinline__ u32 chunk(u32 id) { return id * 2048u + (id >= 20u ? 40960u : 0u); }
__device__ __forceinline__ v4u ld128(u32 a) { return *(const LAS v4u*)(size_t)a; }
__device__ __forceinline__ v2u ld64(u32 a) { return *(const LAS v2u*)(size_t)a; }
__device__ __forceinline__ float bperm(u32 a, float v) { return __uint_as_float((u32)__builtin_amdgcn_ds_bpermute((int)a, (int)__float_as_uint(v))); }
__device__ __forceinline__ float dot2(u32 a, u32 b, float c) { return __builtin_amdgcn_fdot2_f32_bf16(__builtin_bit_cast(v2bf, a), __builtin_bit_cast(v2bf, b), c, false); }
#define RD 2
#define SB() __builtin_amdgcn_sched_barrier(0)
template <int I, int E, class F> __device__ __forceinline__ void sfor(F&& fn) {
  if constexpr (I < E) { fn(std::integral_constant<int, I>{}); sfor<I + 1, E>(fn); }
}
#define fmx(a, b) __builtin_amdgcn_fmed3f((a), (b), finf)
#define PS1 24
#define PS2 48
#define FO(mm) ((mm) == 0 ? 39 : 38 + 4 * (mm) - ((mm) == 7))
#define OPQ(x) asm volatile("" : "+v"(x))
#define OPQ2(x, y) asm volatile("" : "+v"(x), "+v"(y))
__device__ __forceinline__ float sel(bool c, float a, float b) { asm("" : "+v"(a)); return c ? a : b; }
__device__ __forceinline__ u32 pk2o(float a, float b) { asm("" : "+v"(a)); asm("" : "+v"(b)); return pk2(a, b); }
struct TfPre { u32 aT, aR, so, bp, bq; };
__device__ __forceinline__ TfPre tfpre(const u32 j0, const u32 slot, const u32 lane) {
  const u32 g = lane >> 4, n = lane & 15, k = n >> 1, nd = n & 1, u = j0 >> 2, kr = 2 * g + ((n >> 2) & 1);
  TfPre p;
  p.aT = chunk(u * 8 + k) + (g ^ ((k >> 2) << 1)) * 64 + ((j0 ^ k) & 3) * 16 + nd * 8;
  p.aR = chunk(u * 8 + kr) + ((n & 3) ^ ((kr >> 2) << 1)) * 64 + ((j0 ^ kr) & 3) * 16 + (n >> 3) * 8;
  p.so = (slot * 40 + 2 * j0) * 256;
  p.bp = (20 * g + nd) * 4; p.bq = (16 * (n >> 2) + n) * 4;
  OPQ2(p.aT, p.aR); OPQ2(p.bp, p.bq);
  return p;
}
template <int NT, int PQ>
__device__ __forceinline__ void tf(const TfPre& P, const u32 lane, const float dv0, const float dv1, u16* __restrict__ out) {
  const u32 g = lane >> 4, n = lane & 15, k = n >> 1, nd = n & 1;
  const u32 aT = P.aT, bp = P.bp, bq = P.bq;
  u32 aW = lane * 16 + L_WM;
  float finf = __builtin_inff(); asm("" : "+s"(finf));
  v4u TF[NT][4], W[RD];
#pragma unroll
  for (int s = 0; s < 4; s++) {
#pragma unroll
    for (int t = 0; t < NT; t++) {
      const v2u a = ld64((aT ^ (16 * t)) + 512 * s), b = ld64((aT ^ (16 * t)) + 512 * s + 256);
      TF[t][s] = v4u{a[0], a[1], b[0], b[1]};
    }
    if (s < RD) W[s] = ld128(aW + L_WR + s * 1024);
  }
  SB();
  u32 aR[2] = {P.aR, P.aR ^ 16};
  OPQ(aR[1]);
  const float dve[2] = {dv0, dv1};
  float rho[NT], rb[NT][2], c1[NT][2], c3[NT][2], v0[NT], v1[NT], mx[2], zs[2], l1[2], l3[2], q2[2][2];
  v4f S[NT], LN[NT], GR[NT], U[NT][8], y[NT][8], o[NT][8];
  v4u ub[NT], B[NT], YQ[NT][4]; v2u Y[NT][8], tr[NT][8]; u32 dg0[NT], dg1[NT];
  const v4f Z4 = {0, 0, 0, 0};
  float dr; u32 wfl;
  SB();
  sfor<-4, 68>([&](auto Fc) __attribute__((always_inline)) {
    constexpr int f = decltype(Fc)::value, m = (f >> 2) & 7, s = f & 3, fn = f + RD;
    if constexpr (f == -4) __builtin_amdgcn_s_setprio(PQ & 3);
    if constexpr (f == PS1) __builtin_amdgcn_s_setprio((PQ >> 4) & 3);
    if constexpr (f == PS2) __builtin_amdgcn_s_setprio((PQ >> 8) & 3);
    if constexpr (f >= 10 && f <= 34 && (f & 7) == 2) {
#pragma unroll
      for (int t = 0; t < NT; t++) S[t] = MFMA(TF[t][(f - 10) >> 3], ub[t], (f > 10 ? S[t] : Z4));
      SB();
    }
#pragma unroll
    for (int mm = 0; mm < 8; mm++) {
      if (f == FO(mm)) {
#pragma unroll
        for (int t = 0; t < NT; t++) o[t][mm] = MFMA((v4u{tr[t][mm][0], tr[t][mm][1], Y[t][mm][0], Y[t][mm][1]}), B[t], Z4);
        SB();
      }
    }
    if constexpr (f < 64) {
      const v4u wf = W[(f + 4) % RD];
#pragma unroll
      for (int t = 0; t < NT; t++) {
        if constexpr (f < 0) { GR[t] = MFMA(TF[t][s], TF[t][s], (s ? GR[t] : Z4)); LN[t] = MFMA(wf, TF[t][s], (s ? LN[t] : Z4)); }
        else if constexpr (f < 32) U[t][m] = MFMA(wf, TF[t][s], (s ? U[t][m] : Z4));
        else y[t][m] = MFMA(TF[t][s], wf, (s ? y[t][m] : Z4));
      }
      if constexpr (fn == 30) wfl = *(volatile LAS u32*)(size_t)L_FLAG;
      if constexpr (fn == 32) {
        u32 sa, sc;
        asm volatile("s_mov_b32 %2, 0\n"
                     "L_wf%=:\n\tv_readfirstlane_b32 %1, %0\n\ts_add_u32 %2, %2, 1\n\ts_lshr_b32 %1, %1, 2\n\ts_lshr_b32 vcc_lo, %2, 17\n\ts_or_b32 %1, %1, vcc_lo\n\ts_cbranch_scc1 L_wd%=\n"
                     "\tv_mov_b32 %0, %3\n\tds_read_b32 %0, %0\n\ts_waitcnt lgkmcnt(0)\n\ts_branch L_wf%=\n"
                     "L_wd%=:" : "+v"(wfl), "=&s"(sa), "=&s"(sc) : "n"(L_FLAG) : "vcc", "scc", "memory");
        aW += L_WW; OPQ(aW);
      }
      if constexpr (fn < 64)
      W[(f + 4) % RD] = ld128(aW + (fn < 0 ? L_WR + (fn + 4) * 1024 : (fn & 31) * 1024));
    }
#pragma unroll
    for (int t = 0; t < NT; t++) {
      if (f == 1 + 2 * t) {
        const float qa = sel(nd, GR[t][1], GR[t][0]), qb = sel(nd, GR[t][3], GR[t][2]), q = (n & 2) ? qb : qa;
        const float a = LN[t][0] * (1.f / 128.f);
        rho[t] = fmaxf(q * (1.f / 128.f) - a * a, 0.f) + (k == 0 ? 1e-5f : 1e-5f * dve[t] * dve[t]);
        OPQ(rho[t]);
      }
      if (f == 2 + 2 * t) {
        rho[t] = __builtin_amdgcn_rsqf(rho[t]); rho[t] = k == 0 ? rho[t] : __builtin_copysignf(rho[t], dve[t]);
        const float dd = g < 2 ? (k == 0 ? dve[t] : 1.f) : 0.f;
        l1[t] = 0.5f * dd * LN[t][0]; l3[t] = dd * LN[t][1];
        OPQ2(l1[t], l3[t]);
      }
      if (f == 6 + t) {
        rb[t][0] = bperm(bp, rho[t]); rb[t][1] = bperm(bp + 8, rho[t]); rho[t] = bperm(bq, rho[t]);
      }
      if (f == 8 + t) {
        const u32 d1 = k == 0 ? __float_as_uint(dve[t]) : 0x3f800000u, d2 = (k >> 1) == g ? d1 : 0u, dg = d2 >> ((n & 2) ? 0 : 16);
        dg0[t] = nd ? 0u : dg; dg1[t] = nd ? dg : 0u; OPQ2(dg0[t], dg1[t]);
      }
      if (f == 41) { c1[t][0] = bperm(bp, LN[t][2]); c1[t][1] = bperm(bp + 8, LN[t][2]); c3[t][0] = bperm(bp, LN[t][3]); c3[t][1] = bperm(bp + 8, LN[t][3]); }
      if (f == 12) { v0[t] = rb[t][0] * rho[t]; v1[t] = rb[t][1] * rho[t]; OPQ2(v0[t], v1[t]); rb[t][0] *= dve[t]; rb[t][1] *= dve[t]; OPQ2(rb[t][0], rb[t][1]); }
    }
    if (f == 13) { dr = NT == 2 ? sel(g & 1, dv1, dv0) : dv0; OPQ(dr); }
    if constexpr (f >= 5 && f <= 33 && s == 1) {
      constexpr int mm = (f - 5) >> 2;
#pragma unroll
      for (int t = 0; t < NT; t++) { ub[t][(mm & 1) * 2] = pk2(U[t][mm][0], U[t][mm][1]); ub[t][(mm & 1) * 2 + 1] = pk2(U[t][mm][2], U[t][mm][3]); }
    }
    if (f == 35) {
#pragma unroll
      for (int t = 0; t < NT; t++) { v0[t] *= sel(nd, S[t][1], S[t][0]); v1[t] *= sel(nd, S[t][3], S[t][2]); OPQ2(v0[t], v1[t]); mx[t] = fmx(v0[t], v1[t]); }
      if (NT == 1) mx[1] = mx[0];
      sw16(mx[0], mx[1]); mx[0] = fmx(mx[0], mx[1]);
    }
    if (f == 36) { mx[1] = mx[0]; sw32(mx[0], mx[1]); mx[0] = fmx(mx[0], mx[1]); mx[1] = mx[0]; if (NT == 2) sw16(mx[0], mx[1]); }
    if (f == 36) {
#pragma unroll
      for (int t = 0; t < NT; t++) { v0[t] = __builtin_amdgcn_exp2f(v0[t] - mx[t]); v1[t] = __builtin_amdgcn_exp2f(v1[t] - mx[t]); zs[t] = v0[t] + v1[t]; }
      if (NT == 1) zs[1] = zs[0];
    }
    if (f == 37) { sw16(zs[0], zs[1]); zs[0] += zs[1]; zs[1] = zs[0]; sw32(zs[0], zs[1]); zs[0] = __builtin_amdgcn_rcpf(zs[0] + zs[1]); zs[1] = zs[0]; if (NT == 2) sw16(zs[0], zs[1]); }
    if (f == 38) {
#pragma unroll
      for (int t = 0; t < NT; t++) {
        v0[t] *= rb[t][0]; v1[t] *= rb[t][1]; OPQ(v0[t]); v0[t] *= zs[t]; v1[t] *= zs[t];
        const u32 p = pk2o(v0[t], v1[t]);
        B[t] = v4u{dg0[t], dg1[t], nd ? 0u : p, nd ? p : 0u};
      }
    }
    if (f == 44) {
#pragma unroll
      for (int t = 0; t < NT; t++) {
        l1[t] += v0[t] * c1[t][0]; l3[t] += v0[t] * c3[t][0]; OPQ2(l1[t], l3[t]); l1[t] += v1[t] * c1[t][1]; l3[t] += v1[t] * c3[t][1];
        OPQ2(l1[t], l3[t]);
      }
    }
    if (f == 48) {
#pragma unroll
      for (int t = 0; t < NT; t++) { sw32(l1[t], l3[t]); l1[t] += l3[t]; }
      if (NT == 1) l1[1] = l1[0];
      sw16(l1[0], l1[1]); l1[0] += l1[1];
    }
    if (f == 52) { l3[0] = l1[0]; sw32(l1[0], l3[0]); l1[0] *= 1.f / 128.f; OPQ2(l1[0], l3[0]); }
#pragma unroll
    for (int mm = 0; mm < 8; mm++) {
      const int fo = FO(mm);
      if (f == fo - 3) {
#pragma unroll
        for (int t = 0; t < NT; t++) tr[t][mm] = __builtin_bit_cast(v2u, rd(aR[t] + 256 * mm));
      }
      if (f == 37 + 4 * mm - (mm == 7)) {
#pragma unroll
        for (int t = 0; t < NT; t++) {
          Y[t][mm] = v2u{pk2(y[t][mm][0], y[t][mm][2]), pk2(y[t][mm][1], y[t][mm][3])};
          u32 ya = Y[t][mm][0], yb = Y[t][mm][1];
          asm volatile("v_mov_b32 %0, %2\n\tv_mov_b32 %1, %3" : "=&v"(YQ[t][mm >> 1][2 * (mm & 1)]), "=v"(YQ[t][mm >> 1][2 * (mm & 1) + 1]) : "v"(ya), "v"(yb));
        }
      }
#pragma unroll
      for (int h = 0; h < 2; h++) {
        if (f == fo + 1 + h) {
#pragma unroll
          for (int t = 0; t < NT; t++) {
            const float a = o[t][mm][2 * h], c = o[t][mm][2 * h + 1];
            if (mm == 0 && h == 0) { q2[t][0] = a * a; q2[t][1] = c * c; } else { q2[t][0] += a * a; q2[t][1] += c * c; }
            OPQ2(q2[t][0], q2[t][1]);
          }
        }
      }
    }
    SB();
  });
#pragma unroll
  for (int m = 0; m < 8; m++)
#pragma unroll
    for (int t = 0; t < NT; t++) tr[t][m] = __builtin_bit_cast(v2u, rd(aR[t] + 256 * m));
  u32 ln2 = lane; OPQ(ln2);
  const u32 oo = (ln2 & 1) * 256 + (ln2 & 14) * 16 + (ln2 >> 4) * 8;
  float al;
  {
    float qa = q2[0][0] + q2[0][1], qb = q2[NT - 1][0] + q2[NT - 1][1];
    sw16(qa, qb); qa += qb; qb = qa; sw32(qa, qb);
    const float var = fmaxf((qa + qb) * (1.f / 128.f) - l1[0] * l1[0], 0.f);
    const float lg = l3[0] * __builtin_copysignf(__builtin_amdgcn_rsqf(var + 1e-5f * dr * dr), dr);
    float mm = fmx(lg, DPPF(lg, 0x122)); mm = fmx(mm, DPPF(mm, 0x124)); mm = fmx(mm, DPPF(mm, 0x128));
    const float e = __builtin_amdgcn_exp2f(lg - mm);
    float z = e + DPPF(e, 0x122); z += DPPF(z, 0x124); z += DPPF(z, 0x128);
    al = e * __builtin_amdgcn_rcpf(z);
  }
  const float ald = al * __builtin_amdgcn_rcpf(dr), als = k == 0 ? al : ald;
  float aa[2] = {ald, ald}; if (NT == 2) sw16(aa[0], aa[1]);
  const u32 br = (4 * g + nd) * 4;
  v4f R[NT][4];
#pragma unroll
  for (int t = 0; t < NT; t++) {
    const float a0 = bperm(br + 64 * t, als), a1 = bperm(br + 64 * t + 8, als);
    const u32 p = B[t][2] | B[t][3];
    float b0 = bflo(p) * aa[t], b1 = bfhi(p) * aa[t];
    OPQ(b0);
    b0 += DPPF(b0, 0x122); b1 += DPPF(b1, 0x122); b0 += DPPF(b0, 0x124); b1 += DPPF(b1, 0x124); b0 += DPPF(b0, 0x128); b1 += DPPF(b1, 0x128);
    const u32 xa = pk2o(a0, a1), xb = pk2o(b0, b1), r = n & 3;
    const v4u ga = {r == 0 ? xa : 0u, r == 1 ? xa : 0u, r == 2 ? xa : 0u, r == 3 ? xa : 0u};
    const v4u gb = {r == 0 ? xb : 0u, r == 1 ? xb : 0u, r == 2 ? xb : 0u, r == 3 ? xb : 0u};
#pragma unroll
    for (int c = 0; c < 4; c++) R[t][c] = MFMA((v4u{tr[t][2 * c][0], tr[t][2 * c][1], tr[t][2 * c + 1][0], tr[t][2 * c + 1][1]}), ga, Z4);
#pragma unroll
    for (int c = 0; c < 4; c++) R[t][c] = MFMA(YQ[t][c], gb, R[t][c]);
  }
#pragma unroll
  for (int t = 0; t < NT; t++) {
    u32 r0 = pk2(R[t][0][0], R[t][0][1]), r1 = pk2(R[t][0][2], R[t][0][3]);
#pragma unroll
    for (int c = 1; c < 4; c++) {
      u32 s0 = pk2(R[t][c][0], R[t][c][1]), s1 = pk2(R[t][c][2], R[t][c][3]);
      OPQ2(s0, s1);
      r0 = (n >> 2) == c ? s0 : r0; r1 = (n >> 2) == c ? s1 : r1;
    }
    __builtin_amdgcn_raw_buffer_store_b64(v2u{r0, r1}, __builtin_amdgcn_make_buffer_rsrc(out, 0, NN * 256, 0x00020000), oo + 512 * t, P.so, 0);
  }
}
#define P3A 0x123
#define P3B 0x123
#define P3C 0x123

__device__ __forceinline__ v16bf rd4(u32 a0, u32 a1, u32 a2, u32 a3) {
  const v4bf a = rd(a0), b = rd(a1), c = rd(a2), d = rd(a3);
  return __builtin_shufflevector(__builtin_shufflevector(a, b, 0, 1, 2, 3, 4, 5, 6, 7), __builtin_shufflevector(c, d, 0, 1, 2, 3, 4, 5, 6, 7),
                                 0, 1, 2, 3, 4, 5, 6, 7, 8, 9, 10, 11, 12, 13, 14, 15);
}
#define DMA(voff, soff, lbase) asm volatile("s_mov_b32 m0, %2\n\ts_nop 0\n\tbuffer_load_dwordx4 %0, %3, %1 offen sc1 lds" :: "v"(voff), "s"(soff), "s"(__builtin_amdgcn_readfirstlane(lbase)), "s"(srd) : "memory")
#define PFM 1
#define PR1 0
#define PR2 2
#define PR3 4
#define ELLAUX 16
#define PF0 tokdma(w);
#define PF1
#define PF2 tokdma(w + 16);
#define PF3 tokdma(w + 32);
#define PF4 if (w < 12) tokdma(w + 48);
#define NPRE 1
#define KMAX 7
#define LASTP 7
#define D23POS 0
#define D4POS 2
#define T2PRE
__global__ void __launch_bounds__(1024, 4) k2(const u16* __restrict__ dis, const u16* __restrict__ wq, const u16* __restrict__ wk, const u16* __restrict__ wv,
                                           const u16* __restrict__ wo, const u16* __restrict__ wr, const u16* __restrict__ x, char* __restrict__ ws, u16* __restrict__ out) {
#define KID 2
  __shared__ __attribute__((aligned(16))) char T[163840];
  const int t = threadIdx.x, lane = t & 63, w = __builtin_amdgcn_readfirstlane(t >> 6);
  const int b = blockIdx.x, gp = (b & 7) * 2 + (b >> 7), p = (b >> 3) & 15;
  const int slot = p * 16 + w;
  const bool act = slot < 250;
  asm volatile("" :: "v"((u32)(size_t)(LAS char*)T));
  const __amdgpu_buffer_rsrc_t rs = __builtin_amdgcn_make_buffer_rsrc(ws + WS_G, 0, 8u * GBUF, 0x00020000);
  const v4u srd = {(u32)(size_t)ws, (u32)((size_t)ws >> 32) & 0xffffu, WS_DBG, 0x00020000u};
  const u32 sgA = gp * 2 * GSTRIDE;
  const u32 vla = w * 1024 + lane * 16;
  const u32 vlc = (w == 15 && lane >= 40) ? OOB : vla;
  const u32 lb0 = w * 1024, lb2 = w * 1024 + 32768 + (w >= 8 ? 40960 : 0);
  const int bs = p * 16 + gp;
  const u32 vpz = w < 10 ? (w >= 5 ? GSTRIDE : 0u) + ((w % 5) >> 1) * 32768 + p * (w % 5 < 4 ? 2048 : 1024) + ((w % 5) & 1) * 1024 + lane * 16
                         : (w == 12 ? (lane < 17 ? WS_W + ((b >> 3) * 17 + lane) * 128 : OOB) : (lane & 31) * GSTRIDE + (w - 13) * 32768 + min(bs, 249) * (w < 15 ? 128 : 64));
  auto touch = [&](const u32 so) __attribute__((always_inline)) {
    asm volatile("s_mov_b32 m0, 0x27f80\n\ts_mov_b32 exec_hi, 0\n\tbuffer_load_dword %0, %1, %2 offen sc1 lds\n\ts_mov_b32 exec_hi, -1" :: "v"(vpz), "s"(srd), "s"(so) : "memory");
  };
  auto early = [&]() __attribute__((always_inline)) {
    DMA(vla, sgA, lb0); DMA(vla, sgA + 16384, lb0 + 16384); DMA(vla, sgA + 32768, lb2); DMA(vla, sgA + 49152, lb0 + 90112); DMA(vlc, sgA + 65536, lb0 + 106496);
  };
  auto early2 = [&]() __attribute__((always_inline)) {
    if (w < 10) {
      const v4u pz = {POISON, POISON, POISON, POISON};
      DMA(vpz, sgA + GBUF, 156672);
      asm volatile("buffer_store_dwordx4 %0, %1, %2, %3 offen sc1" :: "v"(pz), "v"(vpz), "s"(srd), "s"(sgA + 2 * GBUF) : "memory");
    }
  };
  {
    if (bs < 250) build(bs, ws, (u32*)(T + 40960), early, early2 WSA);
    else {
      if (bs < 252) wprep(bs - 250, wq, wk, wv, wo, wr, ws, (float*)(T + 40960), [&]() { early(); early2(); } WSA);
      else { early(); early2(); asm volatile("s_waitcnt vmcnt(0)" ::: "memory"); }
    }
  }
  if (t < 32) { const v4u z = {0, 0, 0, 0}; *(LAS v4u*)(size_t)(ZROW * 8 + (t >> 4) * 40960 + (t & 15) * 16) = z; }
  LBAR();
  v4u e[5]; u16 dvr[3];
  const int sl = act ? slot : 0;
  dvr[0] = dis[sl * 40 + (lane >> 2)]; dvr[1] = dis[sl * 40 + 16 + (lane >> 2)]; dvr[2] = dis[sl * 40 + 32 + ((lane & 31) >> 2)];
  const __amdgpu_buffer_rsrc_t rw = __builtin_amdgcn_make_buffer_rsrc(ws + WS_ELL, 0, WS_W - WS_ELL, 0x00020000);
  v2u hd = __builtin_amdgcn_raw_buffer_load_b64(rw, 0, WS_HDR - WS_ELL + sl * HS, 16);
#pragma unroll
  for (int i = 0; i < 5; i++) e[i] = __builtin_amdgcn_raw_buffer_load_b128(rw, lane * 16 + i * 1024, sl * 5120, ELLAUX);
  SB();
  u32 vp01 = act ? slot * 128 + (lane >> 1) * 4 : OOB;
  u32 vp2 = act ? 65536 + slot * 64 + (lane >> 1) * 4 : OOB;
  v8bf sel1, sel2; const int seli = (lane & 1) ? 0xEEEEEEEE : 0x44444444;
  {
    const u32 on = (lane & 2) ? 0u : 0x3f803f80u, lo = (lane & 4) ? 0u : on, hi = (lane & 4) ? on : 0u;
    const bool m1 = ((lane >> 4) & 1) == ((lane >> 3) & 1);
    const v4u a2 = {lo, lo, hi, hi}, a1 = {m1 ? lo : 0u, m1 ? lo : 0u, m1 ? hi : 0u, m1 ? hi : 0u};
    sel1 = __builtin_bit_cast(v8bf, a1); sel2 = __builtin_bit_cast(v8bf, a2);
  }
  u32 vchk;
  {
    const u32 i = (lane >> 3) & 3, ln = (w == 15 && (i & 1)) ? (lane & 1) : (lane & 7);
    const u32 hl = (w == 15 && (lane & 15) >= 10) ? (lane & 7) : (lane & 15);
    vchk = lane < 32 ? (i == 2 ? lb2 : lb0 + (i == 1 ? 16384 : (i == 3 ? 90112 : 0))) + ln * 128 + lane * 4 : lb0 + 106496 + hl * 64 + (lane & 15) * 4;
  }
  asm volatile("" : "+v"(vp01), "+v"(vp2), "+v"(vchk), "+v"(sel1), "+v"(sel2));
  for (u32 spins = 0;; spins++) {
    u32 m = max(hd[0], hd[1]);
#pragma unroll
    for (int i = 0; i < 5; i++) m = max(max(m, max(e[i][0], e[i][1])), max(e[i][2], e[i][3]));
    if (__builtin_amdgcn_ballot_w64(m == POISON) == 0 || spins >= MAXSPIN) break;
    __builtin_amdgcn_s_sleep(2);
    asm volatile("" ::: "memory");
    hd = __builtin_amdgcn_raw_buffer_load_b64(rw, 0, WS_HDR - WS_ELL + sl * HS, 16);
#pragma unroll
    for (int i = 0; i < 5; i++) e[i] = __builtin_amdgcn_raw_buffer_load_b128(rw, lane * 16 + i * 1024, sl * 5120, 16);
  }
  const u32 novf = act ? min((u32)__builtin_amdgcn_readfirstlane(hd[1]), (u32)EE) : 0u;
  const u32 ovb = WS_OVF2 - WS_ELL + min((u32)__builtin_amdgcn_readfirstlane(hd[0]), (u32)EE) * 4;
  u32 ad[32];
#pragma unroll
  for (int i = 0; i < 16; i++) { const u32 q = e[i >> 2][i & 3]; ad[2 * i] = (q & 0xffffu) * 8; ad[2 * i + 1] = (q >> 16) * 8; }
  u32 ex[4] = {e[4][0], e[4][1], e[4][2], e[4][3]};
  bool xm;
  {
    const u32 m0 = min(min(ex[0] & 0xffffu, ex[0] >> 16), min(ex[1] & 0xffffu, ex[1] >> 16));
    const u32 m1 = min(min(ex[2] & 0xffffu, ex[2] >> 16), min(ex[3] & 0xffffu, ex[3] >> 16));
    xm = __builtin_amdgcn_ballot_w64(min(m0, m1) < ZROW) != 0;
  }
  float dv2[3];
#pragma unroll
  for (int i = 0; i < 3; i++) { const float d = bf2f(dvr[i]); dv2[i] = d * d; }
  auto slow = [&](const u32 tofs, const u32 n0, const u32 nn) __attribute__((always_inline)) -> float {
    float a = 0.f;
    for (u32 base = 0; base < novf; base += 64) {
      const u32 en = base + lane < novf ? __builtin_amdgcn_raw_buffer_load_b32(rw, (base + lane) * 4, ovb, 16) : POISON;
      u64 m = __builtin_amdgcn_ballot_w64((en >> 16) - n0 < nn);
      while (m) {
        const int l = __builtin_ctzll(m); m &= m - 1;
        const u32 es = __builtin_amdgcn_readlane(en, l);
        const float val = bf2f(*(LAS u16*)(size_t)((es & 0xffffu) * 8 + tofs + (lane & 3) * 2));
        a += ((es >> 16) - n0 == (u32)(lane >> 2)) ? val : 0.f;
      }
    }
    return a;
  };
  auto grow = [&](const u32 la, const u32 scur) __attribute__((always_inline)) -> v2u {
    v2u v = {0, 0};
    if (la < ZROW * 8) {
      u32 spins = 0;
      do {
        v = __builtin_amdgcn_raw_buffer_load_b64(rs, la >= 81920 ? la - 40960 : la, scur, 16);
        asm volatile("" ::: "memory");
      } while (max(v[0], v[1]) == POISON && ++spins < MAXSPIN);
    }
    return v;
  };
  auto fallback = [&](const int bt, const u32 scur) __attribute__((always_inline)) -> float {
    float s0 = 0.f, s1 = 0.f, s2 = 0.f, s3 = 0.f;
#pragma unroll 1
    for (int j = 0; j < (bt < 2 ? 16 : 8); j++) {
      const int je = j < 12 ? bt * 12 + j : 20 + bt * 4 + j;
      const u32 la = __builtin_amdgcn_raw_buffer_load_b16(rw, (je >> 3) * 1024 + lane * 16 + (je & 7) * 2, sl * 5120, 16) * 8u;
      const v2u v = grow(la, scur);
      s0 += bflo(v[0]); s1 += bfhi(v[0]); s2 += bflo(v[1]); s3 += bfhi(v[1]);
    }
    float a = 0.f;
#pragma unroll
    for (int c = 0; c < 4; c++) {
      const int sl_ = (lane & 48) + 4 * c + ((lane >> 2) & 3);
      const float t0 = __shfl(s0, sl_), t1 = __shfl(s1, sl_), t2 = __shfl(s2, sl_), t3 = __shfl(s3, sl_);
      a += (lane & 2) ? ((lane & 1) ? t3 : t2) : ((lane & 1) ? t1 : t0);
    }
    if (bt == 2) a += __shfl_xor(a, 32);
    const u32 n0 = slot * 40 + bt * 16;
    for (u32 i = 0; i < novf; i++) {
      const u32 es = __builtin_amdgcn_raw_buffer_load_b32(rw, i * 4, ovb, 16);
      if ((es >> 16) - n0 == (u32)(lane >> 2) && (bt < 2 || lane < 32)) {
        const v2u v = grow((es & 0xffffu) * 8, scur);
        const u32 q = (lane & 2) ? v[1] : v[0];
        a += (lane & 1) ? bfhi(q) : bflo(q);
      }
    }
    return a;
  };
  auto tail = [&](const int bt, const v4f acc, const u32 scur, const u32 voff, const u32 soff) __attribute__((always_inline)) {
    float a = acc[0] + acc[1];
    if (__builtin_expect(__builtin_amdgcn_ballot_w64(a != a) != 0, 0)) a = fallback(bt, scur);
    const float d2 = dv2[bt];
    const float tn = __builtin_bit_cast(float, DPPU(__float_as_uint(a), 0xB1)) * d2;
    const u32 cm = min(pk2(a * d2, tn), 0xFFFFFFFEu);
    asm volatile("s_mov_b64 exec, %4\n\tbuffer_store_dword %0, %1, %2, %3 offen sc1\n\ts_mov_b64 exec, -1"
                 :: "v"(cm), "v"(voff), "s"(srd), "s"(soff), "s"(bt < 2 ? 0x5555555555555555ull : 0x55555555ull) : "memory");
  };
  v4f acc = {0, 0, 0, 0}, acc2 = {0, 0, 0, 0};
  u32 vp2x = OOB;
  auto tokdma = [&](u32 id) __attribute__((always_inline)) {
    u32 lane = t;
    asm volatile("" : "+s"(id), "+v"(lane));
    lane &= 63;
    const u32 k = ((id * 205u) >> 11) + 1, rem = id - (k - 1) * 10, u = rem >> 1, h = rem & 1;
    const u32 vo = ((lane >> 2) ^ ((k >> 2) << 1)) * GSTRIDE + ((lane ^ k) & 3) * 16;
    const u32 so = WS_G + k * GBUF + h * 16 * GSTRIDE + (u < 4 ? (u >> 1) * 32768 + 128 * bs + (u & 1) * 64 : 65536 + 64 * bs);
    DMA(vo, so, chunk(u * 8 + k) + h * 1024);
  };
  const bool rdo = bs < 250;
  auto phase = [&](auto GBc, auto XMc, const bool recv, const u32 sprev, const u32 scur, const u32 srecv_, const int sb) __attribute__((always_inline)) {
    const u32 srecv = srecv_;
    constexpr u32 GB = decltype(GBc)::value;
    constexpr bool XM = decltype(XMc)::value;
    constexpr u32 to = GB * 40960u, ro = (1 - GB) * 40960u;
    constexpr int NQ = XM ? 10 : 8, E0 = XM ? 3 : 2, E1 = XM ? 7 : 5;
    const bool pois = GB == 0 && scur < sgA + (LASTP - 2) * GBUF && w < 10;
    __builtin_amdgcn_s_setprio(3);
    u32 flg = 0;
#define FLW (w == 0 && l < 18)
    auto vfl = [&]() { u32 l = lane; OPQ(l); return FLW ? WS_FLG + (l < 16 ? p * 16 + l : 250 + (l & 1)) * 4 : OOB; };
    if (GB == 0 && scur == sgA + 5 * GBUF && w == 0) {
      u32 l = lane; OPQ(l);
      asm volatile("buffer_store_dword %0, %0, %1, %2 offen sc1" :: "v"(l == 0 ? 0u : OOB), "s"(srd), "s"(WS_FLG + bs * 4) : "memory");
    }
    if (GB == 0 && scur == sgA + 6 * GBUF) asm volatile("buffer_load_dword %0, %1, %2, 0 offen sc1" : "=v"(flg) : "v"(vfl()), "s"(srd) : "memory");
    if (GB == 1 && !recv && rdo) { PF0 }
    if (recv) { DMA(vla, srecv, lb0 + ro); DMA(vla, srecv + 16384, lb0 + 16384 + ro); }
    auto grp = [&](const int s) __attribute__((always_inline)) -> v16bf {
      if (XM && (s == E0 || s == E1)) {
        u32 q0 = ex[s == E0 ? 0 : 2], q1 = ex[s == E0 ? 1 : 3];
        asm volatile("" : "+v"(q0), "+v"(q1));
        return rd4((q0 & 0xffffu) * 8 + to, (q0 >> 16) * 8 + to, (q1 & 0xffffu) * 8 + to, (q1 >> 16) * 8 + to);
      }
      const int q = XM ? s - (s > E0) - (s > E1) : s;
      return rd4(ad[4 * q] + to, ad[4 * q + 1] + to, ad[4 * q + 2] + to, ad[4 * q + 3] + to);
    };
    v16bf g[3];
#pragma unroll
    for (int q = 0; q < 3; q++) g[q] = grp(q);
    __builtin_amdgcn_sched_barrier(0);
    if (D23POS < 0 && recv) { DMA(vla, srecv + 32768, lb2 + ro); DMA(vla, srecv + 49152, lb0 + 90112 + ro); }
    float e0 = 0.f, e1 = 0.f, e2 = 0.f;
    if (__builtin_expect(novf != 0, 0)) { e0 = slow(to, slot * 40, 16); e1 = slow(to, slot * 40 + 16, 16); e2 = slow(to, slot * 40 + 32, 8); }
    __builtin_amdgcn_sched_barrier(0);
    u32 chk = 0;
#pragma unroll
    for (int q = 0; q < NQ; q++) {
      if (q == 0) { acc[0] = e0; acc[1] = 0.f; }
      if (q == E0 + 1) { acc[0] = e1; acc[1] = 0.f; }
      if (q == E1 + 1) { acc2[0] = e2; acc2[1] = 0.f; }
      if (q == NQ - 1 && recv) {
        asm volatile("s_waitcnt vmcnt(%0)" :: "n"(D4POS <= E0 ? 2 : 1) : "memory");
        chk = *(volatile LAS u32*)(size_t)(vchk + ro);
      }
      if (q <= E1) acc = __builtin_amdgcn_smfmac_f32_16x16x64_bf16(sel1, g[q % 3], acc, seli, 0, 0);
      else acc2 = __builtin_amdgcn_smfmac_f32_16x16x64_bf16(sel2, g[q % 3], acc2, seli, 0, 0);
      if (q + 3 < NQ) g[q % 3] = grp(q + 3);
      if (q == D23POS && recv) { DMA(vla, srecv + 32768, lb2 + ro); DMA(vla, srecv + 49152, lb0 + 90112 + ro); }
      if (q == D4POS && recv) DMA(vlc, srecv + 65536, lb0 + 106496 + ro);
      if (GB == 1 && !recv && rdo) { if (q == 0) { PF1 } }
      if (q == PR1) __builtin_amdgcn_s_setprio(2);
      if (q == PR2) __builtin_amdgcn_s_setprio(1);
      if (q == PR3) __builtin_amdgcn_s_setprio(0);
      __builtin_amdgcn_sched_barrier(0);
      if (q == E0 || q == E1) {
        tail(q == E1, acc, scur, vp01, scur + GBUF + (q == E1 ? 32768 : 0));
        if (GB == 1 && !recv && rdo) { if (q == E0) { PF2 } else { PF3 } }
        __builtin_amdgcn_sched_barrier(0);
      }
    }
    auto again = [&](const u64 bad) __attribute__((always_inline)) {
      if (bad & 0xffull) DMA(vla, srecv, lb0 + ro);
      if (bad & 0xff00ull) DMA(vla, srecv + 16384, lb0 + 16384 + ro);
      if (bad & 0xff0000ull) DMA(vla, srecv + 32768, lb2 + ro);
      if (bad & 0xff000000ull) DMA(vla, srecv + 49152, lb0 + 90112 + ro);
      if (bad >> 32) DMA(vlc, srecv + 65536, lb0 + 106496 + ro);
    };
    u64 bad = __builtin_amdgcn_ballot_w64(chk == POISON);
    if (__builtin_expect(bad != 0, 0)) again(bad);
    tail(2, acc2, scur, vp2, scur + GBUF);
    if (pois) {
      const v4u pz = {POISON, POISON, POISON, POISON};
      asm volatile("buffer_store_dwordx4 %0, %1, %2, %3 offen sc1" :: "v"(pz), "v"(vpz), "s"(srd), "s"(scur + 3 * GBUF) : "memory");
    }
    if (recv) {
      u32 spins = 0;
      if (GB == 0) {
        asm volatile("" : "+v"(flg));
        for (u32 sp = 0; __builtin_amdgcn_ballot_w64(flg == POISON) != 0 && sp < MAXSPIN; sp++)
          asm volatile("buffer_load_dword %0, %1, %2, 0 offen sc1\n\ts_waitcnt vmcnt(0)" : "=v"(flg) : "v"(vfl()), "s"(srd) : "memory");
      }
      if (__builtin_expect(bad != 0, 0)) {
        if (pois) asm volatile("s_waitcnt vmcnt(2)" ::: "memory"); else asm volatile("s_waitcnt vmcnt(1)" ::: "memory");
        bad = __builtin_amdgcn_ballot_w64(*(volatile LAS u32*)(size_t)(vchk + ro) == POISON);
        spins++;
        while (bad != 0 && spins < MAXSPIN) {
          spins++;
          again(bad);
          asm volatile("s_waitcnt vmcnt(0)" ::: "memory");
          bad = __builtin_amdgcn_ballot_w64(*(volatile LAS u32*)(size_t)(vchk + ro) == POISON);
        }
      }
      asm volatile("" ::: "memory");
      __builtin_amdgcn_s_barrier();
      asm volatile("" ::: "memory");
    }
  };
  using F0 = std::integral_constant<u32, 0>; using F1 = std::integral_constant<u32, 1>;
  auto hops = [&](auto XMc) __attribute__((always_inline)) {
    u32 sk = sgA;
#pragma unroll 1
    for (int k = 1; k <= KMAX; k++) {
      phase(F0{}, XMc, true, sk - GBUF + GSTRIDE, sk, sk + GSTRIDE, 6 * k - 4);
      phase(F1{}, XMc, k < KMAX, sk, sk + GSTRIDE, sk + GBUF, 6 * k - 1);
      sk += GBUF;
    }
  };
  if (xm) hops(std::true_type{}); else hops(std::false_type{});
  if (rdo) {
    PF4
    v4u xv = {0, 0, 0, 0};
    if (t < 640) xv = *(const v4u*)(x + (size_t)bs * 5120 + t * 8);
    const u32 j0 = w < 8 ? 2 * w : min(w + 8, 19), two = w < 8 ? 2 : 0;
    const u16 dq0 = dis[bs * 40 + 2 * j0 + (lane & 1)], dq1 = dis[bs * 40 + 2 * j0 + two + (lane & 1)];
    LBAR();
    DMA(lane * 16, WS_W + w * 1024, L_WM + w * 1024); DMA(lane * 16, WS_W + 16384 + w * 1024, L_WM + 16384 + w * 1024);
    if (w < 4) DMA(lane * 16, WS_W + 65536 + w * 1024, L_WM + L_WR + w * 1024);
    if (t == 0) *(volatile LAS u32*)(size_t)L_FLAG = 0;
    if (w >= 6) tokdma(w + 54);
    if (t < 640) {
      const u32 node = t >> 4, a = chunk((node >> 3) * 8) + (t & 15) * 128 + ((node >> 1) & 3) * 16 + (node & 1) * 8;
      *(LAS v2u*)(size_t)a = v2u{xv[0], xv[1]}; *(LAS v2u*)(size_t)(a + 64) = v2u{xv[2], xv[3]};
    }
    const TfPre P = tfpre(j0, bs, lane);
    float dv0 = bf2f(dq0), dv1 = bf2f(dq1);
    dv0 = dv0 == 0.f ? 1.f : dv0; dv1 = dv1 == 0.f ? 1.f : dv1; OPQ2(dv0, dv1);
    for (u32 spins = 0;; spins++) {
      asm volatile("s_waitcnt vmcnt(0)" ::: "memory");
      u32 bad = 0; v4u cv[5];
#pragma unroll
      for (int i = 0; i < 5; i++) {
        const u32 id = i < 3 ? w + 16 * i : (i == 3 ? (w < 12 ? w + 48 : w) : (w >= 6 ? w + 54 : w));
        const u32 k = ((id * 205u) >> 11) + 1, rem = id - (k - 1) * 10;
        cv[i] = ld128(chunk((rem >> 1) * 8 + k) + (rem & 1) * 1024 + lane * 16);
      }
      SB();
#pragma unroll
      for (int i = 0; i < 5; i++) bad |= __builtin_amdgcn_ballot_w64(max(max(cv[i][0], cv[i][1]), max(cv[i][2], cv[i][3])) == POISON) != 0 ? 1u << i : 0u;
      if (bad == 0 || spins >= MAXSPIN) break;
#pragma unroll
      for (int i = 0; i < 5; i++)
        if ((bad >> i) & 1) tokdma(i < 3 ? w + 16 * i : (i == 3 ? (w < 12 ? w + 48 : w) : (w >= 6 ? w + 54 : w)));
    }
    LBAR();
    if (w < 8) tf<2, P3A>(P, lane, dv0, dv1, out);
    else if (w < 12) tf<1, P3C>(P, lane, dv0, dv1, out);
    else {
#pragma unroll
      for (int i = 0; i < 8; i++) DMA(lane * 16, WS_W + 32768 + (i * 4 + w - 12) * 1024, L_WM + L_WW + (i * 4 + w - 12) * 1024);
      asm volatile("s_waitcnt vmcnt(0)" ::: "memory");
      if (lane == 0) __hip_atomic_fetch_add((LAS u32*)(size_t)L_FLAG, 1u, __ATOMIC_RELAXED, __HIP_MEMORY_SCOPE_WORKGROUP);
    }
  }
  asm volatile("L_k2_last:");
#undef KID
}
__device__ __host__ __forceinline__ u32 rowof(u32 n) { u32 s = n / 40, l = n - s * 40; return l < 32 ? (l >> 4) * 4096 + 16 * s + (l & 15) : 8192 - 32 + 8 * s + l; }

extern "C" void kernel_launch(void* const* d_in, const int* in_sizes, int n_in, void* d_out, int out_size, void* d_ws, size_t ws_size, hipStream_t stream) {
  const u16* x = (const u16*)d_in[0]; const int* src = (const int*)d_in[1]; const int* dst = (const int*)d_in[2]; const u16* dis = (const u16*)d_in[3];
  const u16* wq = (const u16*)d_in[4]; const u16* wk = (const u16*)d_in[5]; const u16* wv = (const u16*)d_in[6]; const u16* wo = (const u16*)d_in[7]; const u16* wr = (const u16*)d_in[8];
  char* ws = (char*)d_ws;
  k1<<<253, 640, 0, stream>>>(src, dst, x, dis, wq, wk, wv, wo, ws, (const u16*)d_out);
  k2<<<256, 1024, 0, stream>>>(dis, wq, wk, wv, wo, wr, x, ws, (u16*)d_out);
}
#define WS_TOK WS_G
#define TOKBYTES (8u * GBUF)
#define TOKSC(n) hbf2f(dis[n])
#define TOKADDR(n,k,d) ((size_t)(k) * GBUF + ((d) >> 2) * GSTRIDE + rowof(n) * 8 + ((d) & 3) * 2)
```
